```python
import torch

def _softmax(x, axis=-1):
    return torch.softmax(x, dim=axis)
def _gelu(x):
    return torch.nn.functional.gelu(x, approximate="tanh")
def _silu(x):
    return torch.nn.functional.silu(x)
def _sigmoid(x):
    return torch.sigmoid(x)
def _layernorm(x, axis=-1, eps=1e-5):
    m = x.mean(dim=axis, keepdim=True)
    v = x.var(dim=axis, keepdim=True, unbiased=False)
    return (x - m) / torch.sqrt(v + eps)
def _scatter_add(tgt, idx, src, axis=0):
    return tgt.clone().index_add_(axis, idx, src)
def _segment_sum(src, seg, n):
    out = torch.zeros((n,) + src.shape[1:], dtype=src.dtype)
    return out.index_add_(0, seg, src)
def _pad(x, pw):
    flat = tuple(v for pair in reversed(pw) for v in pair)
    return torch.nn.functional.pad(x, flat)

kernel_name = 'gated_mix__gqa_block_cached__geglu_ffn__decode'

def setup_inputs():

    _w = (lambda a: (lambda t: t.to(torch.bfloat16) if t.is_floating_point() else t)(torch.as_tensor(a)))
    def _A_setup():
        B, Sq, D = 1, 1, 448
        Hq, Hkv, Sk, Dh = 16, 8, 65536, 256
        assert Hq % Hkv == 0, (Hq, Hkv)
        return {
            "x": _w((lambda *s: torch.randn(*s, dtype=torch.bfloat16) * 0.05)(B, Sq, D)),
            "k_cache": _w((lambda *s: torch.randn(*s, dtype=torch.bfloat16) * 6.4)(B, Hkv, Sk, Dh)),
            "v_cache": _w((lambda *s: torch.randn(*s, dtype=torch.bfloat16) * 0.2)(B, Hkv, Sk, Dh)),
            "wq": _w((lambda *s: torch.randn(*s, dtype=torch.bfloat16) * 6.4)(D, Hq * Dh) * (D ** -0.5)),
            "wo": _w((lambda *s: torch.randn(*s, dtype=torch.bfloat16) * 0.2)(Hq * Dh, D) * ((Hq * Dh) ** -0.5)),
        }
    _A_in = _A_setup()

    def _B_setup():
        B, S, D, F = 1, 1, 448, 1792
        return {
            "x": _w((lambda *s: torch.randn(*s, dtype=torch.bfloat16) * 0.05)(B, S, D)),
            "w_gate": _w((lambda *s: torch.randn(*s, dtype=torch.bfloat16) * 0.05)(D, F)),
            "w_up": _w((lambda *s: torch.randn(*s, dtype=torch.bfloat16) * 0.05)(D, F)),
            "w_down": _w((lambda *s: torch.randn(*s, dtype=torch.bfloat16) * 0.05)(F, D)),
        }
    _B_in = _B_setup()

    return {
        "A_x": _A_in["x"],
        "A_k_cache": _A_in["k_cache"],
        "A_v_cache": _A_in["v_cache"],
        "A_wq": _A_in["wq"],
        "A_wo": _A_in["wo"],
        "B_x": _B_in["x"],
        "B_w_gate": _B_in["w_gate"],
        "B_w_up": _B_in["w_up"],
        "B_w_down": _B_in["w_down"],
        "gate_w": _w((lambda *s: torch.randn(*s, dtype=torch.bfloat16) * 0.05)(448) * 16.0),
    }

def reference(A_x, A_k_cache, A_v_cache, A_wq, A_wo, B_x, B_w_gate, B_w_up, B_w_down, gate_w):
    def _A_ref(x, k_cache, v_cache, wq, wo):
        B, Sq, D = x.shape
        Hq, Dh = 16, 256
        Hkv = k_cache.shape[1]
        rep = Hq // Hkv
        q = (lambda x, *d: x.permute(*d))((x @ wq).reshape(B, Sq, Hq, Dh), 0, 2, 1, 3)
        k = (lambda x, n, axis: torch.repeat_interleave(x, n, dim=axis))(k_cache, rep, axis=1)
        v = (lambda x, n, axis: torch.repeat_interleave(x, n, dim=axis))(v_cache, rep, axis=1)
        scores = torch.einsum("bhqd,bhkd->bhqk", q, k) * (Dh ** -0.5)
        p = _softmax(scores, axis=-1)
        o = torch.einsum("bhqk,bhkd->bhqd", p, v)
        return (lambda x, *d: x.permute(*d))(o, 0, 2, 1, 3).reshape(B, Sq, Hq * Dh) @ wo

    def _B_ref(x, w_gate, w_up, w_down):
        return (_gelu(x @ w_gate) * (x @ w_up)) @ w_down

    x = A_x
    g = _sigmoid((x * gate_w).sum(-1, keepdims=True))
    a = _A_ref(x, A_k_cache, A_v_cache, A_wq, A_wo)
    b = _B_ref(x, B_w_gate, B_w_up, B_w_down)
    return g * a + (1 - g) * b
```

```cpp
#include <hip/hip_runtime.h>
#include <hip/hip_bf16.h>
#include <cstdint>

using bf16x2 = __attribute__((ext_vector_type(2))) __bf16;
using u32x4  = __attribute__((ext_vector_type(4))) unsigned;
using f32x4  = __attribute__((ext_vector_type(4))) float;

constexpr int D = 448, HQ = 16, HKV = 8, SK = 65536, DH = 256, F = 1792;
constexpr int NSPLIT = 32;
constexpr int TOK_PER_WG = SK / NSPLIT;
constexpr int NW = 8;
constexpr int TILE = 16;

__device__ __forceinline__ float bf2f(unsigned short b) { return __uint_as_float((unsigned)b << 16); }
__device__ __forceinline__ unsigned short f2bf(float f) { __hip_bfloat16 h = __float2bfloat16(f); return *reinterpret_cast<unsigned short*>(&h); }
__device__ __forceinline__ float rbf(float f) { return bf2f(f2bf(f)); }
__device__ __forceinline__ float dot2(unsigned k, unsigned q, float acc) {
  return __builtin_amdgcn_fdot2_f32_bf16(__builtin_bit_cast(bf16x2, k), __builtin_bit_cast(bf16x2, q), acc, false);
}

constexpr size_t WS_Q = 0, WS_H = 8192, WS_G = 16384, WS_PML = 32768, WS_PO = 65536;

__global__ void __launch_bounds__(256) k_prep(const unsigned short* x, const unsigned short* wq, const unsigned short* wg, const unsigned short* wu,
                       const unsigned short* gate_w, unsigned char* ws) {
  __shared__ float xs[D];
  for (int i = threadIdx.x; i < D; i += 256) xs[i] = bf2f(x[i]);
  __syncthreads();
  int b = blockIdx.x;
  if (b < 16) {
    int n = b * 256 + threadIdx.x;
    float acc = 0.f;
    for (int k = 0; k < D; ++k) acc = fmaf(xs[k], bf2f(wq[(size_t)k * 4096 + n]), acc);
    reinterpret_cast<unsigned short*>(ws + WS_Q)[n] = f2bf(acc);
  } else if (b < 23) {
    int n = (b - 16) * 256 + threadIdx.x;
    float a = 0.f, u = 0.f;
    for (int k = 0; k < D; ++k) { a = fmaf(xs[k], bf2f(wg[(size_t)k * F + n]), a); u = fmaf(xs[k], bf2f(wu[(size_t)k * F + n]), u); }
    a = rbf(a); u = rbf(u);
    float t = tanhf(0.7978845608028654f * (a + 0.044715f * a * a * a));
    float ge = rbf(0.5f * a * (1.f + t));
    reinterpret_cast<float*>(ws + WS_H)[n] = rbf(ge * u);
  } else {
    if (threadIdx.x == 0) {
      float s = 0.f;
      for (int k = 0; k < D; ++k) s += rbf(xs[k] * bf2f(gate_w[k]));
      s = rbf(s);
      reinterpret_cast<float*>(ws + WS_G)[0] = rbf(1.f / (1.f + __expf(-s)));
    }
  }
}

template <int CTRL> __device__ __forceinline__ float dpp(float x) {
  return __builtin_bit_cast(float, __builtin_amdgcn_mov_dpp(__builtin_bit_cast(int, x), CTRL, 0xf, 0xf, true));
}
constexpr int XOR1 = 0xB1, XOR2 = 0x4E, XOR7 = 0x141, XOR8 = 0x128;

__global__ void __launch_bounds__(NW * 64) k_attn(const unsigned short* kc, const unsigned short* vc, unsigned char* ws) {
  __shared__ __attribute__((aligned(16))) float lds_p[NW][2][16];
  __shared__ __attribute__((aligned(16))) float mrg[NW * 2][2][4 + 256];
  const int h = blockIdx.x & 7, s = blockIdx.x >> 3;
  const int tid = threadIdx.x, lane = tid & 63, wave = __builtin_amdgcn_readfirstlane(tid >> 6);
  const int tp = lane >> 5, ch = lane & 31;
  const unsigned short* qv = reinterpret_cast<const unsigned short*>(ws + WS_Q);
  u32x4 q[2];
  q[0] = *reinterpret_cast<const u32x4*>(qv + (2 * h) * 256 + 8 * ch);
  q[1] = *reinterpret_cast<const u32x4*>(qv + (2 * h + 1) * 256 + 8 * ch);
  const size_t base = ((size_t)h * SK + (size_t)s * TOK_PER_WG) * DH;
  const u32x4* kp = reinterpret_cast<const u32x4*>(kc + base) + lane;
  const u32x4* vp = reinterpret_cast<const u32x4*>(vc + base) + lane;
  constexpr int NT = TOK_PER_WG / TILE / NW;
  constexpr float C2 = 0.0625f * 1.4426950408889634f;
  float m[2] = {-INFINITY, -INFINITY};
  float l = 0.f;
  float acc[2][8];
#pragma unroll
  for (int r = 0; r < 2; ++r)
#pragma unroll
    for (int i = 0; i < 8; ++i) acc[r][i] = 0.f;
  u32x4 Kn[8], Vn[8];
  {
    const int t = wave;
#pragma unroll
    for (int j = 0; j < 8; ++j) { Kn[j] = __builtin_nontemporal_load(kp + (size_t)t * 512 + j * 64); }
#pragma unroll
    for (int j = 0; j < 8; ++j) { Vn[j] = __builtin_nontemporal_load(vp + (size_t)t * 512 + j * 64); }
  }
  for (int it = 0; it < NT; ++it) {
    u32x4 Kc[8], Vc[8];
#pragma unroll
    for (int j = 0; j < 8; ++j) { Kc[j] = Kn[j]; Vc[j] = Vn[j]; }
    if (it + 1 < NT) {
      const int t = (it + 1) * NW + wave;
#pragma unroll
      for (int j = 0; j < 8; ++j) { Kn[j] = __builtin_nontemporal_load(kp + (size_t)t * 512 + j * 64); }
#pragma unroll
      for (int j = 0; j < 8; ++j) { Vn[j] = __builtin_nontemporal_load(vp + (size_t)t * 512 + j * 64); }
    }
    float x[16];
#pragma unroll
    for (int j = 0; j < 8; ++j)
#pragma unroll
      for (int r = 0; r < 2; ++r) {
        float d = 0.f;
#pragma unroll
        for (int i = 0; i < 4; ++i) d = dot2(Kc[j][i], q[r][i], d);
        x[j * 2 + r] = d;
      }
    const int c = lane & 15;
#define TR_STEP(HALF, CTRL, BIT) _Pragma("unroll") for (int hh = 0; hh < (HALF); ++hh) { \
      const float lo_ = x[hh], hi_ = x[hh + (HALF)]; const float t1_ = lo_ + dpp<CTRL>(lo_), t2_ = hi_ + dpp<CTRL>(hi_); \
      x[hh] = (c & (BIT)) ? t2_ : t1_; }
    TR_STEP(8, XOR8, 8) TR_STEP(4, XOR7, 4) TR_STEP(2, XOR2, 2) TR_STEP(1, XOR1, 1)
#undef TR_STEP
    float sc = x[0];
    sc += __shfl_xor(sc, 16);
    sc *= C2;
    float pm = sc;
    pm = fmaxf(pm, dpp<XOR8>(pm)); pm = fmaxf(pm, dpp<0x124>(pm));   pm = fmaxf(pm, dpp<XOR2>(pm));
    const float pm_o = dpp<XOR1>(pm);
    const float pm0 = (c & 1) ? pm_o : pm, pm1 = (c & 1) ? pm : pm_o;
    const float mn0 = fmaxf(m[0], pm0), mn1 = fmaxf(m[1], pm1);
    const float a0 = exp2f(m[0] - mn0), a1 = exp2f(m[1] - mn1);
    m[0] = mn0; m[1] = mn1;
    const float p = exp2f(sc - ((c & 1) ? mn1 : mn0));
    l = l * ((c & 1) ? a1 : a0) + p;
#pragma unroll
    for (int i = 0; i < 8; ++i) { acc[0][i] *= a0; acc[1][i] *= a1; }
    if ((lane & 16) == 0) lds_p[wave][tp][c] = p;
    asm volatile("s_waitcnt lgkmcnt(0)" ::: "memory");
    f32x4 pr[4];
#pragma unroll
    for (int u = 0; u < 4; ++u) pr[u] = *reinterpret_cast<const f32x4*>(&lds_p[wave][tp][4 * u]);
#pragma unroll
    for (int j = 0; j < 8; ++j) {
      const float p0 = pr[j >> 1][(j & 1) * 2], p1 = pr[j >> 1][(j & 1) * 2 + 1];
#pragma unroll
      for (int i = 0; i < 4; ++i) {
        const float lo = __uint_as_float(Vc[j][i] << 16), hi = __uint_as_float(Vc[j][i] & 0xffff0000u);
        acc[0][2 * i] = fmaf(p0, lo, acc[0][2 * i]); acc[0][2 * i + 1] = fmaf(p0, hi, acc[0][2 * i + 1]);
        acc[1][2 * i] = fmaf(p1, lo, acc[1][2 * i]); acc[1][2 * i + 1] = fmaf(p1, hi, acc[1][2 * i + 1]);
      }
    }
    asm volatile("" ::: "memory");
  }
  {
    float t = l;
    t += dpp<XOR8>(t); t += dpp<0x124>(t); t += dpp<XOR2>(t);
    l = t;
  }
  const int slot = wave * 2 + tp;
#pragma unroll
  for (int r = 0; r < 2; ++r) {
    *reinterpret_cast<f32x4*>(&mrg[slot][r][4 + 8 * ch]) = f32x4{acc[r][0], acc[r][1], acc[r][2], acc[r][3]};
    *reinterpret_cast<f32x4*>(&mrg[slot][r][4 + 8 * ch + 4]) = f32x4{acc[r][4], acc[r][5], acc[r][6], acc[r][7]};
  }
  if (ch < 2) { mrg[slot][ch][0] = m[ch]; mrg[slot][ch][1] = l; }
  __syncthreads();
  {
    const int r = tid >> 8, d = tid & 255;
    float M = -INFINITY;
    for (int w = 0; w < NW * 2; ++w) M = fmaxf(M, mrg[w][r][0]);
    float L = 0.f, o = 0.f;
    for (int w = 0; w < NW * 2; ++w) { const float wt = exp2f(mrg[w][r][0] - M); L = fmaf(wt, mrg[w][r][1], L); o = fmaf(wt, mrg[w][r][4 + d], o); }
    float* pml = reinterpret_cast<float*>(ws + WS_PML);
    float* po = reinterpret_cast<float*>(ws + WS_PO);
    const int row = (2 * h + r) * NSPLIT + s;
    po[(size_t)row * 256 + d] = o;
    if (d == 0) { pml[row * 2] = M; pml[row * 2 + 1] = L; }
  }
}

__global__ void __launch_bounds__(256) k_final(const unsigned short* wo, const unsigned short* wd, const unsigned char* ws, unsigned short* out) {
  __shared__ float o[4096];
  __shared__ float red[256][9];
  const float* pml = reinterpret_cast<const float*>(ws + WS_PML);
  const float* po = reinterpret_cast<const float*>(ws + WS_PO);
  const int tid = threadIdx.x;
  for (int hh = 0; hh < HQ; ++hh) {
    float M = -INFINITY;
    for (int s = 0; s < NSPLIT; ++s) M = fmaxf(M, pml[(hh * NSPLIT + s) * 2]);
    float L = 0.f, a = 0.f;
    for (int s = 0; s < NSPLIT; ++s) { const float wt = exp2f(pml[(hh * NSPLIT + s) * 2] - M); L = fmaf(wt, pml[(hh * NSPLIT + s) * 2 + 1], L); a = fmaf(wt, po[(size_t)(hh * NSPLIT + s) * 256 + tid], a); }
    o[hh * 256 + tid] = rbf(a / L);
  }
  __syncthreads();
  const int n0 = blockIdx.x * 8;
  float a[8], b[8];
#pragma unroll
  for (int i = 0; i < 8; ++i) { a[i] = 0.f; b[i] = 0.f; }
  for (int k = tid; k < 4096; k += 256) {
    const u32x4 w = *reinterpret_cast<const u32x4*>(wo + (size_t)k * D + n0);
#pragma unroll
    for (int i = 0; i < 4; ++i) { a[2 * i] = fmaf(o[k], __uint_as_float(w[i] << 16), a[2 * i]); a[2 * i + 1] = fmaf(o[k], __uint_as_float(w[i] & 0xffff0000u), a[2 * i + 1]); }
  }
  const float* hv = reinterpret_cast<const float*>(ws + WS_H);
  for (int k = tid; k < F; k += 256) {
    const u32x4 w = *reinterpret_cast<const u32x4*>(wd + (size_t)k * D + n0);
#pragma unroll
    for (int i = 0; i < 4; ++i) { b[2 * i] = fmaf(hv[k], __uint_as_float(w[i] << 16), b[2 * i]); b[2 * i + 1] = fmaf(hv[k], __uint_as_float(w[i] & 0xffff0000u), b[2 * i + 1]); }
  }
  for (int pass = 0; pass < 2; ++pass) {
    __syncthreads();
#pragma unroll
    for (int i = 0; i < 8; ++i) red[tid][i] = pass ? b[i] : a[i];
    __syncthreads();
    if (tid < 8) {
      float sacc = 0.f;
      for (int t = 0; t < 256; ++t) sacc += red[t][tid];
      red[0][tid] = sacc;
      if (pass == 0) a[0] = sacc; else b[0] = sacc;
    }
  }
  if (tid < 8) {
    const float g = reinterpret_cast<const float*>(ws + WS_G)[0];
    const float av = rbf(a[0]), bv = rbf(b[0]);
    const float r = rbf(rbf(g * av) + rbf(rbf(1.f - g) * bv));
    out[n0 + tid] = f2bf(r);
  }
}

extern "C" void kernel_launch(void* const* d_in, const int* in_sizes, int n_in, void* d_out, int out_size, void* d_ws, size_t ws_size, hipStream_t stream) {
  const unsigned short* x = (const unsigned short*)d_in[0];
  const unsigned short* kc = (const unsigned short*)d_in[1];
  const unsigned short* vc = (const unsigned short*)d_in[2];
  const unsigned short* wq = (const unsigned short*)d_in[3];
  const unsigned short* wo = (const unsigned short*)d_in[4];
  const unsigned short* wg = (const unsigned short*)d_in[6];
  const unsigned short* wu = (const unsigned short*)d_in[7];
  const unsigned short* wd = (const unsigned short*)d_in[8];
  const unsigned short* gw = (const unsigned short*)d_in[9];
  unsigned char* ws = (unsigned char*)d_ws;
  k_prep<<<24, 256, 0, stream>>>(x, wq, wg, wu, gw, ws);
  k_attn<<<HKV * NSPLIT, NW * 64, 0, stream>>>(kc, vc, ws);
  k_final<<<56, 256, 0, stream>>>(wo, wd, ws, (unsigned short*)d_out);
}
```

```cpp
#include <hip/hip_runtime.h>
#include <hip/hip_bf16.h>
#include <cstdint>

using bf16x2 = __attribute__((ext_vector_type(2))) __bf16;
using bf16x8 = __attribute__((ext_vector_type(8))) __bf16;
using s16x4  = __attribute__((ext_vector_type(4))) short;
using u32x4  = __attribute__((ext_vector_type(4))) unsigned;
using u32x2  = __attribute__((ext_vector_type(2))) unsigned;
using f32x4  = __attribute__((ext_vector_type(4))) float;
using f32x2  = __attribute__((ext_vector_type(2))) float;
#define LAS __attribute__((address_space(3)))

constexpr int D = 448, HQ = 16, HKV = 8, SK = 65536, DH = 256, F = 1792;
constexpr int NSPLIT = 32;
constexpr int TOK_PER_WG = SK / NSPLIT;
constexpr int NW = 4;
constexpr int NT = TOK_PER_WG / 16 / NW;

__device__ __forceinline__ float bf2f(unsigned short b) { return __uint_as_float((unsigned)b << 16); }
__device__ __forceinline__ unsigned short f2bf(float f) { __hip_bfloat16 h = __float2bfloat16(f); return *reinterpret_cast<unsigned short*>(&h); }
__device__ __forceinline__ float rbf(float f) { return bf2f(f2bf(f)); }
__device__ __forceinline__ unsigned cvtpk(float lo, float hi) { f32x2 v = {lo, hi}; bf16x2 b = __builtin_convertvector(v, bf16x2); return __builtin_bit_cast(unsigned, b); }

constexpr size_t WS_Q = 0, WS_H = 8192, WS_G = 16384, WS_PML = 32768, WS_PO = 65536;

__global__ void __launch_bounds__(256) k_prep(const unsigned short* x, const unsigned short* wq, const unsigned short* wg, const unsigned short* wu,
                       const unsigned short* gate_w, unsigned char* ws) {
  __shared__ float xs[D];
  for (int i = threadIdx.x; i < D; i += 256) xs[i] = bf2f(x[i]);
  __syncthreads();
  int b = blockIdx.x;
  if (b < 16) {
    int n = b * 256 + threadIdx.x;
    float acc = 0.f;
    for (int k = 0; k < D; ++k) acc = fmaf(xs[k], bf2f(wq[(size_t)k * 4096 + n]), acc);
    reinterpret_cast<unsigned short*>(ws + WS_Q)[n] = f2bf(acc);
  } else if (b < 23) {
    int n = (b - 16) * 256 + threadIdx.x;
    float a = 0.f, u = 0.f;
    for (int k = 0; k < D; ++k) { a = fmaf(xs[k], bf2f(wg[(size_t)k * F + n]), a); u = fmaf(xs[k], bf2f(wu[(size_t)k * F + n]), u); }
    a = rbf(a); u = rbf(u);
    float t = tanhf(0.7978845608028654f * (a + 0.044715f * a * a * a));
    float ge = rbf(0.5f * a * (1.f + t));
    reinterpret_cast<float*>(ws + WS_H)[n] = rbf(ge * u);
  } else {
    if (threadIdx.x == 0) {
      float s = 0.f;
      for (int k = 0; k < D; ++k) s += rbf(xs[k] * bf2f(gate_w[k]));
      s = rbf(s);
      reinterpret_cast<float*>(ws + WS_G)[0] = rbf(1.f / (1.f + __expf(-s)));
    }
  }
}

__device__ __forceinline__ float xrow16_max(float x) {
  auto s = __builtin_amdgcn_permlane16_swap(__float_as_uint(x), __float_as_uint(x), false, false);
  x = fmaxf(__uint_as_float(s[0]), __uint_as_float(s[1]));
  auto t = __builtin_amdgcn_permlane32_swap(__float_as_uint(x), __float_as_uint(x), false, false);
  return fmaxf(__uint_as_float(t[0]), __uint_as_float(t[1]));
}
__device__ __forceinline__ float xrow16_sum(float x) {
  auto s = __builtin_amdgcn_permlane16_swap(__float_as_uint(x), __float_as_uint(x), false, false);
  x = __uint_as_float(s[0]) + __uint_as_float(s[1]);
  auto t = __builtin_amdgcn_permlane32_swap(__float_as_uint(x), __float_as_uint(x), false, false);
  return __uint_as_float(t[0]) + __uint_as_float(t[1]);
}

#define DMA8(m0a, rs, so, v0, v1, v2, v3, v4, v5, v6, v7)                                  \
  asm volatile("s_mov_b32 m0, %0\n\ts_nop 0\n\t"                                           \
               "buffer_load_dwordx4 %4, %2, %3 offen nt lds\n\t"                           \
               "buffer_load_dwordx4 %5, %2, %3 offen offset:1024 nt lds\n\t"               \
               "buffer_load_dwordx4 %6, %2, %3 offen offset:2048 nt lds\n\t"               \
               "buffer_load_dwordx4 %7, %2, %3 offen offset:3072 nt lds\n\t"               \
               :: "s"(m0a), "s"(0), "s"(rs), "s"(so), "v"(v0), "v"(v1), "v"(v2), "v"(v3) : "memory"); \
  asm volatile("s_mov_b32 m0, %0\n\ts_nop 0\n\t"                                           \
               "buffer_load_dwordx4 %4, %2, %3 offen nt lds\n\t"                           \
               "buffer_load_dwordx4 %5, %2, %3 offen offset:1024 nt lds\n\t"               \
               "buffer_load_dwordx4 %6, %2, %3 offen offset:2048 nt lds\n\t"               \
               "buffer_load_dwordx4 %7, %2, %3 offen offset:3072 nt lds\n\t"               \
               :: "s"((m0a) + 4096u), "s"(0), "s"(rs), "s"((so) + 4096u), "v"(v4), "v"(v5), "v"(v6), "v"(v7) : "memory")
#define WAITV(n) asm volatile("s_waitcnt vmcnt(" #n ")" ::: "memory")
#define WAITL0() asm volatile("s_waitcnt lgkmcnt(0)" ::: "memory")

struct SW {
  __amdgpu_buffer_rsrc_t rk, rv;
  unsigned vk[8], vv[4];
  unsigned ka[4], va[8];
  unsigned m0base;
  bf16x8 qf[8];
  f32x4 acc[16];
  float m, l;
};

template <int SLOT, bool ISSUE, int WK, int WV>
__device__ __forceinline__ void tile(SW& c, unsigned so_next) {
  constexpr float C2 = 0.0625f * 1.4426950408889634f;
  constexpr unsigned KOFF = SLOT * 8192u, VOFF = 16384u + SLOT * 8192u;
  if constexpr (WK == 24) WAITV(24); else if constexpr (WK == 8) WAITV(8);
  bf16x8 kf[8];
#pragma unroll
  for (int s = 0; s < 8; ++s) kf[s] = *(LAS bf16x8*)(size_t)(c.ka[s & 3] + KOFF + (s >> 2) * 256u);
  f32x4 s0 = {0.f, 0.f, 0.f, 0.f}, s1 = {0.f, 0.f, 0.f, 0.f};
#pragma unroll
  for (int s = 0; s < 8; s += 2) {
    s0 = __builtin_amdgcn_mfma_f32_16x16x32_bf16(kf[s], c.qf[s], s0, 0, 0, 0);
    s1 = __builtin_amdgcn_mfma_f32_16x16x32_bf16(kf[s + 1], c.qf[s + 1], s1, 0, 0, 0);
  }
  WAITL0();
  if constexpr (ISSUE) { DMA8(c.m0base + KOFF, c.rk, so_next, c.vk[0], c.vk[1], c.vk[2], c.vk[3], c.vk[4], c.vk[5], c.vk[6], c.vk[7]); }
  f32x4 S = s0 + s1;
  float mx = fmaxf(fmaxf(S[0], S[1]), fmaxf(S[2], S[3]));
  mx = xrow16_max(mx);
  if (__any(mx > c.m)) {
    const float mn = fmaxf(c.m, mx);
    const float alpha = __builtin_amdgcn_exp2f((c.m - mn) * C2);
    c.m = mn; c.l *= alpha;
#pragma unroll
    for (int cb = 0; cb < 16; ++cb) c.acc[cb] *= alpha;
  }
  const float mc = c.m * C2;
  const float p0 = __builtin_amdgcn_exp2f(fmaf(S[0], C2, -mc)), p1 = __builtin_amdgcn_exp2f(fmaf(S[1], C2, -mc));
  const float p2 = __builtin_amdgcn_exp2f(fmaf(S[2], C2, -mc)), p3 = __builtin_amdgcn_exp2f(fmaf(S[3], C2, -mc));
  c.l += (p0 + p1) + (p2 + p3);
  u32x2 pk = {cvtpk(p0, p1), cvtpk(p2, p3)};
  const s16x4 P = __builtin_bit_cast(s16x4, pk);
  if constexpr (WV == 24) WAITV(24); else if constexpr (WV == 16) WAITV(16); else WAITV(0);
  s16x4 vf[16];
#pragma unroll
  for (int cb = 0; cb < 16; ++cb) vf[cb] = __builtin_amdgcn_ds_read_tr16_b64_v4i16((LAS s16x4*)(size_t)(c.va[cb & 7] + VOFF + (cb >> 3) * 256u));
#pragma unroll
  for (int cb = 0; cb < 16; ++cb) c.acc[cb] = __builtin_amdgcn_mfma_f32_16x16x16bf16_1k(vf[cb], P, c.acc[cb], 0, 0, 0);
  WAITL0();
  if constexpr (ISSUE) { DMA8(c.m0base + VOFF, c.rv, so_next, c.vv[0], c.vv[1], c.vv[2], c.vv[3], c.vv[0], c.vv[1], c.vv[2], c.vv[3]); }
}

__global__ void __launch_bounds__(NW * 64) k_attn(const unsigned short* kc, const unsigned short* vc, unsigned char* ws) {
  extern __shared__ __attribute__((aligned(16))) unsigned char lds[];
  const int h = blockIdx.x & 7, s = blockIdx.x >> 3;
  const int tid = threadIdx.x, lane = tid & 63, wave = __builtin_amdgcn_readfirstlane(tid >> 6);
  const unsigned ldsb = (unsigned)(size_t)lds;
  SW c;
  const size_t base = ((size_t)h * SK + (size_t)s * TOK_PER_WG) * DH * 2;
  c.rk = __builtin_amdgcn_make_buffer_rsrc((void*)((const char*)kc + base), 0, TOK_PER_WG * DH * 2, 0x00020000);
  c.rv = __builtin_amdgcn_make_buffer_rsrc((void*)((const char*)vc + base), 0, TOK_PER_WG * DH * 2, 0x00020000);
  const unsigned hi = lane >> 5, pc = lane & 31;
#pragma unroll
  for (int j = 0; j < 8; ++j) c.vk[j] = (hi * 512u + ((pc ^ hi) << 4)) ^ (unsigned)(j << 5);
#pragma unroll
  for (int j = 0; j < 4; ++j) c.vv[j] = (hi * 512u + ((pc ^ (hi << 1)) << 4)) ^ (unsigned)(j << 6);
  c.m0base = ldsb + wave * 32768u;
  const unsigned cc = lane & 15, g = lane >> 4;
#pragma unroll
  for (int q = 0; q < 4; ++q) c.ka[q] = c.m0base + cc * 512u + ((g ^ (cc & 3)) << 4) + ((q ^ (cc >> 2)) << 6);
  {
    const unsigned qq = cc >> 2, p = cc & 3, t = 4 * g + qq;
#pragma unroll
    for (int e = 0; e < 8; ++e) c.va[e] = c.m0base + t * 512u + ((e ^ (t & 7)) << 5) + p * 8u;
  }
  unsigned so = wave * 8192u;
  DMA8(c.m0base, c.rk, so, c.vk[0], c.vk[1], c.vk[2], c.vk[3], c.vk[4], c.vk[5], c.vk[6], c.vk[7]);
  DMA8(c.m0base + 16384u, c.rv, so, c.vv[0], c.vv[1], c.vv[2], c.vv[3], c.vv[0], c.vv[1], c.vv[2], c.vv[3]);
  so += NW * 8192u;
  const unsigned short* qv = reinterpret_cast<const unsigned short*>(ws + WS_Q) + (2 * h) * 256;
  u32x4 qraw[8];
#pragma unroll
  for (int st = 0; st < 8; ++st) qraw[st] = *reinterpret_cast<const u32x4*>(qv + (cc & 1) * 256 + 32 * st + 8 * g);
  DMA8(c.m0base + 8192u, c.rk, so, c.vk[0], c.vk[1], c.vk[2], c.vk[3], c.vk[4], c.vk[5], c.vk[6], c.vk[7]);
  DMA8(c.m0base + 24576u, c.rv, so, c.vv[0], c.vv[1], c.vv[2], c.vv[3], c.vv[0], c.vv[1], c.vv[2], c.vv[3]);
  so += NW * 8192u;
  WAITV(16);
#pragma unroll
  for (int st = 0; st < 8; ++st) { u32x4 z = {0u, 0u, 0u, 0u}; asm volatile("" : "+v"(qraw[st])); c.qf[st] = __builtin_bit_cast(bf16x8, cc < 2 ? qraw[st] : z); }
  c.m = -INFINITY; c.l = 0.f;
#pragma unroll
  for (int cb = 0; cb < 16; ++cb) c.acc[cb] = f32x4{0.f, 0.f, 0.f, 0.f};
  for (int it = 0; it < NT - 2; it += 2) {
    tile<0, true, 24, 24>(c, so); so += NW * 8192u;
    tile<1, true, 24, 24>(c, so); so += NW * 8192u;
  }
  tile<0, false, 24, 16>(c, 0);
  tile<1, false, 8, 0>(c, 0);
  const float lsum = xrow16_sum(c.l);
  float* mrg = reinterpret_cast<float*>(lds + wave * 32768u);
  if (cc < 2) {
#pragma unroll
    for (int cb = 0; cb < 16; ++cb) *reinterpret_cast<f32x4*>(mrg + cc * 260 + 4 + 16 * cb + 4 * g) = c.acc[cb];
    if (g == 0) { mrg[cc * 260] = c.m; mrg[cc * 260 + 1] = lsum; }
  }
  __syncthreads();
  {
    constexpr float C2 = 0.0625f * 1.4426950408889634f;
    const int r = tid >> 7, d = (tid & 127) * 2;
    float M = -INFINITY;
#pragma unroll
    for (int w = 0; w < NW; ++w) M = fmaxf(M, reinterpret_cast<const float*>(lds + w * 32768u)[r * 260]);
    float L = 0.f, o0 = 0.f, o1 = 0.f;
#pragma unroll
    for (int w = 0; w < NW; ++w) {
      const float* mw = reinterpret_cast<const float*>(lds + w * 32768u) + r * 260;
      const float wt = __builtin_amdgcn_exp2f((mw[0] - M) * C2);
      L = fmaf(wt, mw[1], L); o0 = fmaf(wt, mw[4 + d], o0); o1 = fmaf(wt, mw[4 + d + 1], o1);
    }
    float* pml = reinterpret_cast<float*>(ws + WS_PML);
    float* po = reinterpret_cast<float*>(ws + WS_PO);
    const int row = (2 * h + r) * NSPLIT + s;
    *reinterpret_cast<f32x2*>(po + (size_t)row * 256 + d) = f32x2{o0, o1};
    if ((tid & 127) == 0) { pml[row * 2] = M * C2; pml[row * 2 + 1] = L; }
  }
}

__global__ void __launch_bounds__(256) k_final(const unsigned short* wo, const unsigned short* wd, const unsigned char* ws, unsigned short* out) {
  __shared__ float o[4096];
  __shared__ float red[256][9];
  const float* pml = reinterpret_cast<const float*>(ws + WS_PML);
  const float* po = reinterpret_cast<const float*>(ws + WS_PO);
  const int tid = threadIdx.x;
  for (int hh = 0; hh < HQ; ++hh) {
    float M = -INFINITY;
    for (int s = 0; s < NSPLIT; ++s) M = fmaxf(M, pml[(hh * NSPLIT + s) * 2]);
    float L = 0.f, a = 0.f;
    for (int s = 0; s < NSPLIT; ++s) { const float wt = exp2f(pml[(hh * NSPLIT + s) * 2] - M); L = fmaf(wt, pml[(hh * NSPLIT + s) * 2 + 1], L); a = fmaf(wt, po[(size_t)(hh * NSPLIT + s) * 256 + tid], a); }
    o[hh * 256 + tid] = rbf(a / L);
  }
  __syncthreads();
  const int n0 = blockIdx.x * 8;
  float a[8], b[8];
#pragma unroll
  for (int i = 0; i < 8; ++i) { a[i] = 0.f; b[i] = 0.f; }
  for (int k = tid; k < 4096; k += 256) {
    const u32x4 w = *reinterpret_cast<const u32x4*>(wo + (size_t)k * D + n0);
#pragma unroll
    for (int i = 0; i < 4; ++i) { a[2 * i] = fmaf(o[k], __uint_as_float(w[i] << 16), a[2 * i]); a[2 * i + 1] = fmaf(o[k], __uint_as_float(w[i] & 0xffff0000u), a[2 * i + 1]); }
  }
  const float* hv = reinterpret_cast<const float*>(ws + WS_H);
  for (int k = tid; k < F; k += 256) {
    const u32x4 w = *reinterpret_cast<const u32x4*>(wd + (size_t)k * D + n0);
#pragma unroll
    for (int i = 0; i < 4; ++i) { b[2 * i] = fmaf(hv[k], __uint_as_float(w[i] << 16), b[2 * i]); b[2 * i + 1] = fmaf(hv[k], __uint_as_float(w[i] & 0xffff0000u), b[2 * i + 1]); }
  }
  for (int pass = 0; pass < 2; ++pass) {
    __syncthreads();
#pragma unroll
    for (int i = 0; i < 8; ++i) red[tid][i] = pass ? b[i] : a[i];
    __syncthreads();
    if (tid < 8) {
      float sacc = 0.f;
      for (int t = 0; t < 256; ++t) sacc += red[t][tid];
      if (pass == 0) a[0] = sacc; else b[0] = sacc;
    }
  }
  if (tid < 8) {
    const float g = reinterpret_cast<const float*>(ws + WS_G)[0];
    const float av = rbf(a[0]), bv = rbf(b[0]);
    const float r = rbf(rbf(g * av) + rbf(rbf(1.f - g) * bv));
    out[n0 + tid] = f2bf(r);
  }
}

extern "C" void kernel_launch(void* const* d_in, const int* in_sizes, int n_in, void* d_out, int out_size, void* d_ws, size_t ws_size, hipStream_t stream) {
  const unsigned short* x = (const unsigned short*)d_in[0];
  const unsigned short* kc = (const unsigned short*)d_in[1];
  const unsigned short* vc = (const unsigned short*)d_in[2];
  const unsigned short* wq = (const unsigned short*)d_in[3];
  const unsigned short* wo = (const unsigned short*)d_in[4];
  const unsigned short* wg = (const unsigned short*)d_in[6];
  const unsigned short* wu = (const unsigned short*)d_in[7];
  const unsigned short* wd = (const unsigned short*)d_in[8];
  const unsigned short* gw = (const unsigned short*)d_in[9];
  unsigned char* ws = (unsigned char*)d_ws;
  static bool attr = false;
  if (!attr) { (void)hipFuncSetAttribute((const void*)k_attn, hipFuncAttributeMaxDynamicSharedMemorySize, NW * 32768); attr = true; }
  k_prep<<<24, 256, 0, stream>>>(x, wq, wg, wu, gw, ws);
  k_attn<<<HKV * NSPLIT, NW * 64, NW * 32768, stream>>>(kc, vc, ws);
  k_final<<<56, 256, 0, stream>>>(wo, wd, ws, (unsigned short*)d_out);
}
```

```cpp
#include <hip/hip_runtime.h>
#include <cstdint>

using bf16x2 = __attribute__((ext_vector_type(2))) __bf16;
using bf16x8 = __attribute__((ext_vector_type(8))) __bf16;
using s16x4  = __attribute__((ext_vector_type(4))) short;
using u32x4  = __attribute__((ext_vector_type(4))) unsigned;
using u32x2  = __attribute__((ext_vector_type(2))) unsigned;
using f32x4  = __attribute__((ext_vector_type(4))) float;
using f32x2  = __attribute__((ext_vector_type(2))) float;
typedef u32x4 u32x4_a4 __attribute__((aligned(4)));
typedef unsigned long long u64;
typedef unsigned short u16;
#define LAS __attribute__((address_space(3)))
#define GAS __attribute__((address_space(1)))
#define CAS __attribute__((address_space(4)))
#define DI __device__ __forceinline__

constexpr int D = 448, HQ = 16, HKV = 8, SK = 65536, DH = 256, F = 1792;
constexpr int NSPLIT = 32;
constexpr int TOK_PER_WG = SK / NSPLIT;
constexpr int NW = 4;
constexpr int NT = TOK_PER_WG / 16 / NW;
constexpr int NWG = HKV * NSPLIT;
constexpr int NC3 = 112;
constexpr float C2 = 0.0625f * 1.4426950408889634f;

#define NB 3
#define BIAS 0
#define MASTER 255
#define FFN_IT 6
#define WO_IT 16
#define QW0 300
#define QW1 30
constexpr size_t WS_E = 0;
constexpr size_t WS_RDY = 4096;
constexpr size_t WS_QG = 8192;
constexpr size_t WS_BC = 40960;
constexpr size_t WS_PROG = 57344;
constexpr size_t WS_IN2 = 65536;
constexpr size_t IN2_STRIDE = 4096;
constexpr size_t WS_Y = WS_IN2 + 256 * IN2_STRIDE;
constexpr size_t WS_TS = WS_Y + 512 * 1024;
constexpr unsigned L_RING = 0;
constexpr unsigned L_Q = 131072;
constexpr unsigned L_RED = L_Q + 1024;
constexpr unsigned L_W = L_RED + 2048;
constexpr unsigned L_WD = L_W + 14336;
constexpr unsigned L_YB = L_WD + 7168;
constexpr unsigned L_FLAG = L_YB + 1856;
constexpr unsigned L_BC = L_FLAG + 64;
constexpr unsigned L_SMP = L_BC + 64;
constexpr unsigned L_TS = L_SMP + 1024;
constexpr unsigned LDS_BYTES = L_TS + 512;
constexpr unsigned EMPTY = 0xFFFFFFFFu;
constexpr unsigned SPIN_G = 1u << 17, SPIN_L = 1u << 22;

DI unsigned cvtpk(float lo, float hi) { f32x2 v = {lo, hi}; bf16x2 b = __builtin_convertvector(v, bf16x2); return __builtin_bit_cast(unsigned, b); }
DI float rbf(float f) { return __uint_as_float(cvtpk(f, 0.f) << 16); }
DI float lo16(unsigned w) { return __uint_as_float(w << 16); }
DI float hi16(unsigned w) { return __uint_as_float(w & 0xffff0000u); }
DI float dot2(unsigned a, unsigned b, float acc) { return __builtin_amdgcn_fdot2_f32_bf16(__builtin_bit_cast(bf16x2, a), __builtin_bit_cast(bf16x2, b), acc, false); }
DI unsigned guard(float f) { unsigned u = __float_as_uint(f); return u < 0xFFFFFFFEu ? u : 0xFFFFFFFEu; }
template <int CTRL> DI float dpp(float x) { return __builtin_bit_cast(float, __builtin_amdgcn_mov_dpp(__builtin_bit_cast(int, x), CTRL, 0xf, 0xf, true)); }
template <int CTRL> DI unsigned dppu(unsigned x) { return (unsigned)__builtin_amdgcn_mov_dpp((int)x, CTRL, 0xf, 0xf, true); }
constexpr int ROR4 = 0x124, ROR8 = 0x128, ROR1 = 0x121, ROR2 = 0x122;
DI float xrow16_max(float x) {
  auto s = __builtin_amdgcn_permlane16_swap(__float_as_uint(x), __float_as_uint(x), false, false);
  x = fmaxf(__uint_as_float(s[0]), __uint_as_float(s[1]));
  auto t = __builtin_amdgcn_permlane32_swap(__float_as_uint(x), __float_as_uint(x), false, false);
  return fmaxf(__uint_as_float(t[0]), __uint_as_float(t[1]));
}
DI float xrow16_sum(float x) {
  auto s = __builtin_amdgcn_permlane16_swap(__float_as_uint(x), __float_as_uint(x), false, false);
  x = __uint_as_float(s[0]) + __uint_as_float(s[1]);
  auto t = __builtin_amdgcn_permlane32_swap(__float_as_uint(x), __float_as_uint(x), false, false);
  return __uint_as_float(t[0]) + __uint_as_float(t[1]);
}
DI float wave_sum(float x) { x += dpp<ROR1>(x); x += dpp<ROR2>(x); x += dpp<ROR4>(x); x += dpp<ROR8>(x); return xrow16_sum(x); }
DI float wave_max(float x) { x = fmaxf(x, dpp<ROR1>(x)); x = fmaxf(x, dpp<ROR2>(x)); x = fmaxf(x, dpp<ROR4>(x)); x = fmaxf(x, dpp<ROR8>(x)); return xrow16_max(x); }

DI u64 next_tag(u64 e) { const u64 t = e + 1ull; return (unsigned)t == 0u ? t + 1ull : t; }

#define LDW(p) __builtin_nontemporal_load(reinterpret_cast<const u32x4*>(p))
#define FPOL "nt"
#define WAITV_(n) asm volatile("s_waitcnt vmcnt(" #n ")" ::: "memory")
#define WAITV(n) WAITV_(n)
#define WAITL0() asm volatile("s_waitcnt lgkmcnt(0)" ::: "memory")
#define BAR() do { asm volatile("s_waitcnt lgkmcnt(0)" ::: "memory"); __builtin_amdgcn_s_barrier(); asm volatile("" ::: "memory"); } while (0)
DI void st_sc1_x4(void* p, u32x4 v) { asm volatile("global_store_dwordx4 %0, %1, off sc1\n\ts_nop 1" :: "v"(p), "v"(v) : "memory"); }
DI void st_sc1_x1(void* p, unsigned v) { asm volatile("global_store_dword %0, %1, off sc1\n\ts_nop 1" :: "v"(p), "v"(v) : "memory"); }
DI unsigned now32() { return (unsigned)__builtin_amdgcn_s_memrealtime(); }
DI void st_sc1_x2(void* p, u32x2 v) { asm volatile("global_store_dwordx2 %0, %1, off sc1\n\ts_nop 1" :: "v"(p), "v"(v) : "memory"); }

#define STAMP(i) do { } while (0)
#define STAMP0(lo, hi) do { } while (0)
#define DUMP(lo, hi) do { } while (0)

#define DMA1(m0a, rs, so, v0)                                                              \
  asm volatile("s_mov_b32 m0, %0\n\ts_nop 0\n\t"                                           \
               "buffer_load_dwordx4 %3, %1, %2 offen nt lds"                               \
               :: "s"(m0a), "s"(rs), "s"(so), "v"(v0) : "memory")
#define DMA1F(m0a, rs, so, v0)                                                             \
  asm volatile("s_mov_b32 m0, %0\n\ts_nop 0\n\t"                                           \
               "buffer_load_dwordx4 %3, %1, %2 offen " FPOL " lds"                         \
               :: "s"(m0a), "s"(rs), "s"(so), "v"(v0) : "memory")
#define DMAC4(m0a, rs, so, v0)                                                             \
  asm volatile("s_mov_b32 m0, %0\n\ts_nop 0\n\t"                                           \
               "buffer_load_dwordx4 %3, %1, %2 offen sc1 lds"                              \
               :: "s"(m0a), "s"(rs), "s"(so), "v"(v0) : "memory")
#define DMAC1(m0a, rs, so, v0)                                                             \
  asm volatile("s_mov_b32 m0, %0\n\ts_nop 0\n\t"                                           \
               "buffer_load_dword %3, %1, %2 offen sc1 lds"                                \
               :: "s"(m0a), "s"(rs), "s"(so), "v"(v0) : "memory")
#define DMA4(m0a, rs, so, v0, v1, v2, v3)                                                  \
  asm volatile("s_mov_b32 m0, %0\n\ts_nop 0\n\t"                                           \
               "buffer_load_dwordx4 %3, %1, %2 offen nt lds\n\t"                           \
               "buffer_load_dwordx4 %4, %1, %2 offen offset:1024 nt lds\n\t"               \
               "buffer_load_dwordx4 %5, %1, %2 offen offset:2048 nt lds\n\t"               \
               "buffer_load_dwordx4 %6, %1, %2 offen offset:3072 nt lds"                   \
               :: "s"(m0a), "s"(rs), "s"(so), "v"(v0), "v"(v1), "v"(v2), "v"(v3) : "memory")

struct SW {
  __amdgpu_buffer_rsrc_t rk, rv;
  unsigned vk[8], vv[4];
  unsigned ka[4], va[8];
  unsigned m0base, tlo;
  bf16x8 qf[8];
  f32x4 acc[16];
  float m, l;
};

DI f32x4 qk_part(const SW& c, unsigned koff) {
  bf16x8 kf[8];
#pragma unroll
  for (int s = 0; s < 8; ++s) kf[s] = *(LAS bf16x8*)(size_t)(c.ka[s & 3] + koff + (s >> 2) * 256u);
  f32x4 s0 = {0.f, 0.f, 0.f, 0.f}, s1 = {0.f, 0.f, 0.f, 0.f};
#pragma unroll
  for (int s = 0; s < 8; s += 2) {
    s0 = __builtin_amdgcn_mfma_f32_16x16x32_bf16(kf[s], c.qf[s], s0, 0, 0, 0);
    s1 = __builtin_amdgcn_mfma_f32_16x16x32_bf16(kf[s + 1], c.qf[s + 1], s1, 0, 0, 0);
  }
  return s0 + s1;
}
DI s16x4 soft_part(SW& c, const f32x4 S) {
  float mx = fmaxf(fmaxf(S[0], S[1]), fmaxf(S[2], S[3]));
  mx = xrow16_max(mx);
  if (__any(mx > c.m)) {
    const float mn = fmaxf(c.m, mx);
    const float alpha = __builtin_amdgcn_exp2f((c.m - mn) * C2);
    c.m = mn; c.l *= alpha;
#pragma unroll
    for (int cb = 0; cb < 16; ++cb) c.acc[cb] *= alpha;
  }
  const float mc = c.m * C2;
  const float p0 = __builtin_amdgcn_exp2f(fmaf(S[0], C2, -mc)), p1 = __builtin_amdgcn_exp2f(fmaf(S[1], C2, -mc));
  const float p2 = __builtin_amdgcn_exp2f(fmaf(S[2], C2, -mc)), p3 = __builtin_amdgcn_exp2f(fmaf(S[3], C2, -mc));
  c.l += (p0 + p1) + (p2 + p3);
  u32x2 pk = {cvtpk(p0, p1), cvtpk(p2, p3)};
  return __builtin_bit_cast(s16x4, pk);
}
DI void pv_part(SW& c, const s16x4 P, unsigned voff) {
  s16x4 vf[16];
#pragma unroll
  for (int cb = 0; cb < 16; ++cb) vf[cb] = __builtin_amdgcn_ds_read_tr16_b64_v4i16((LAS s16x4*)(size_t)(c.va[cb & 7] + voff + (cb >> 3) * 256u));
#pragma unroll
  for (int cb = 0; cb < 16; ++cb) c.acc[cb] = __builtin_amdgcn_mfma_f32_16x16x16bf16_1k(vf[cb], P, c.acc[cb], 0, 0, 0);
}

template <int N> DI void waitv() {
  static_assert(N >= 0 && N <= 28 && (N & 3) == 0, "");
  if constexpr (N == 0) WAITV(0); else if constexpr (N == 4) WAITV(4); else if constexpr (N == 8) WAITV(8); else if constexpr (N == 12) WAITV(12);
  else if constexpr (N == 16) WAITV(16); else if constexpr (N == 20) WAITV(20); else if constexpr (N == 24) WAITV(24); else WAITV(28);
}
template <int Q> DI void issue(const SW& c, unsigned sob) {
  constexpr unsigned part = Q & 3, slot = (Q >> 2) & 1;
  const unsigned so = sob + (unsigned)(Q >> 2) * (NW * 8192u) + (part & 1) * 4096u;
  constexpr unsigned lo = (part >> 1) * 16384u + slot * 8192u + (part & 1) * 4096u;
  if constexpr (part == 0) DMA4(c.m0base + lo, c.rk, so, c.vk[0], c.vk[1], c.vk[2], c.vk[3]);
  else if constexpr (part == 1) DMA4(c.m0base + lo, c.rk, so, c.vk[4], c.vk[5], c.vk[6], c.vk[7]);
  else DMA4(c.m0base + lo, c.rv, so, c.vv[0], c.vv[1], c.vv[2], c.vv[3]);
}
template <int J, int YOUNGER, bool ISSUE> DI void step(SW& c, unsigned sob, s16x4& P) {
  waitv<4 * YOUNGER>();
  if constexpr (ISSUE) issue<J + NB>(c, sob);
  if constexpr ((J & 3) == 1) { const f32x4 S = qk_part(c, (J >> 2) * 8192u); P = soft_part(c, S); WAITL0(); }
  if constexpr ((J & 3) == 3) { pv_part(c, P, 16384u + (J >> 2) * 8192u); WAITL0(); }
}
template <int J> DI void steps_first(SW& c, unsigned sob, s16x4& P) {
  if constexpr (J < 8) { step<J, (J < 8 - NB) ? 7 - J : NB - 1, (J >= 8 - NB)>(c, sob, P); steps_first<J + 1>(c, sob, P); }
}
template <int J> DI void steps_mid(SW& c, unsigned sob, s16x4& P) {
  if constexpr (J < 8) { step<J, NB - 1, true>(c, sob, P); steps_mid<J + 1>(c, sob, P); }
}
template <int J> DI void steps_last(SW& c, unsigned sob, s16x4& P) {
  if constexpr (J < 8) { step<J, (J < 8 - NB) ? NB - 1 : 7 - J, (J < 8 - NB)>(c, sob, P); steps_last<J + 1>(c, sob, P); }
}
template <int J> DI void steps_one(SW& c, unsigned sob, s16x4& P) {
  if constexpr (J < 4) { step<J, (J < 4 - NB) ? NB - 1 : 3 - J, (J < 4 - NB)>(c, sob, P); steps_one<J + 1>(c, sob, P); }
}
template <int J> DI void fill(const SW& c, unsigned sob) {
  if constexpr (J < 8) { if constexpr (J >= NB) waitv<4 * (NB - 1)>(); issue<J>(c, sob); fill<J + 1>(c, sob); }
}

struct Args {
  const u16* x; const u16* kc; const u16* vc; const u16* wq; const u16* wo;
  const u16* wg; const u16* wu; const u16* wd; const u16* gw;
  u16* out; unsigned char* ws;
};

DI const CAS Args* kargs() {
  const CAS Args* p = (const CAS Args*)__builtin_amdgcn_kernarg_segment_ptr();
  asm volatile("" : "+s"(p));
  return p;
}

DI float colsum16(const u32x4 (&w)[14], const float (&xv)[14], float* red, int lane) {
  float acc[8];
#pragma unroll
  for (int j = 0; j < 8; ++j) acc[j] = 0.f;
#pragma unroll
  for (int i = 0; i < 14; ++i)
#pragma unroll
    for (int j = 0; j < 4; ++j) { acc[2 * j] = fmaf(xv[i], lo16(w[i][j]), acc[2 * j]); acc[2 * j + 1] = fmaf(xv[i], hi16(w[i][j]), acc[2 * j + 1]); }
  *reinterpret_cast<f32x4*>(red + (lane >> 1) * 16 + (lane & 1) * 8) = f32x4{acc[0], acc[1], acc[2], acc[3]};
  *reinterpret_cast<f32x4*>(red + (lane >> 1) * 16 + (lane & 1) * 8 + 4) = f32x4{acc[4], acc[5], acc[6], acc[7]};
  WAITL0();
  float t = 0.f;
  const int col = lane & 15, part = lane >> 4;
#pragma unroll
  for (int i = 0; i < 8; ++i) t += red[(part * 8 + i) * 16 + col];
  t = xrow16_sum(t);
  WAITL0();
  return t;
}

__global__ void __launch_bounds__(320, 1) fused_kernel(Args a) {
  extern __shared__ __attribute__((aligned(16))) unsigned char lds[];
  const int b = blockIdx.x;
  const int h = (b >> 3) & 7, s = (b & 7) + 8 * (b >> 6);
  const int xh = b & 7, xs = b >> 3;
  const int me = h * 32 + s;
  const int tid = threadIdx.x, lane = tid & 63, wave = __builtin_amdgcn_readfirstlane(tid >> 6);
  volatile LAS unsigned* flag = (volatile LAS unsigned*)(size_t)((unsigned)(size_t)lds + L_FLAG);
  unsigned char* in2 = a.ws + WS_IN2;
  unsigned char* yb_ = a.ws + WS_Y;
  volatile LAS unsigned* bc = (volatile LAS unsigned*)(size_t)((unsigned)(size_t)lds + L_BC);

  u64 E = *(const CAS u64*)(a.ws + WS_E);
  const unsigned ldsb = (unsigned)(size_t)lds;
  const int u0 = (xh * 28 + (xs < 28 ? xs : 27)) * 8;

  if (wave == NW) {
    __builtin_amdgcn_s_setprio(3);
    const unsigned tstart = now32();
    STAMP0(0, 9);
    STAMP(0);
    const int lc = lane < 56 ? lane : 55;
    const int r0 = (lane >> 1) * 14;
    u32x4 wqr[14], xx, gg, xa, xb;
    {
      const u16* wp = a.wq + (size_t)r0 * 4096 + (xh * 512 + xs * 16 + (lane & 1) * 8);
#pragma unroll
      for (int i = 0; i < 14; ++i) wqr[i] = LDW(wp + (size_t)i * 4096);
      xa = *reinterpret_cast<const u32x4_a4*>(a.x + r0);
      xb = *reinterpret_cast<const u32x4_a4*>(a.x + r0 + 6);
      xx = *reinterpret_cast<const u32x4*>(a.x + lc * 8); gg = *reinterpret_cast<const u32x4*>(a.gw + lc * 8);
    }
    if (lane < 16) flag[lane] = 0u;
    if (lane < 4) bc[lane] = 0u;
    BAR();
    float xv[14];
#pragma unroll
    for (int i = 0; i < 14; ++i) asm volatile("" : "+v"(wqr[i]));
    asm volatile("" : "+v"(xa), "+v"(xb));
#pragma unroll
    for (int i = 0; i < 4; ++i) { xv[2 * i] = lo16(xa[i]); xv[2 * i + 1] = hi16(xa[i]); }
#pragma unroll
    for (int i = 0; i < 3; ++i) { xv[8 + 2 * i] = lo16(xb[1 + i]); xv[9 + 2 * i] = hi16(xb[1 + i]); }
    float* red = reinterpret_cast<float*>(lds + L_RED);
    asm volatile("" : "+s"(E));
    const u64 T = next_tag(E);
    const u32x2 Tw = {(unsigned)T, (unsigned)(T >> 32)};
    {
      const float qv = colsum16(wqr, xv, red, lane);
      const u64 gr = (T << 16) | (u64)(cvtpk(qv, 0.f) & 0xffffu);
      if (lane < 16) st_sc1_x2(a.ws + WS_QG + (size_t)(xh * 512 + xs * 16 + lane) * 8, u32x2{(unsigned)gr, (unsigned)(gr >> 32)});
      if (lane == 0) flag[1] = 1u;
    }
    STAMP(1);
    bool dead = false;
    {
      const unsigned char* qg = a.ws + WS_QG + (size_t)h * 4096 + lane * 16;
      const unsigned tlo = (unsigned)(T << 16), thi = (unsigned)(T >> 16);
      u32x4 g0 = {~tlo, ~thi, ~tlo, ~thi}, g1 = g0, g2 = g0, g3 = g0;
      unsigned spins = 0;
      while (now32() - tstart < (unsigned)QW0) __builtin_amdgcn_s_sleep(2);
      for (;;) {
        auto ok1 = [&](const u32x4& g) { return ((g[0] & 0xffff0000u) == tlo) & (g[1] == thi) & ((g[2] & 0xffff0000u) == tlo) & (g[3] == thi); };
        const bool need = !(ok1(g0) & ok1(g1) & ok1(g2) & ok1(g3));
        if (!__any(need)) break;
        if (++spins > SPIN_G) { dead = true; break; }
        if (spins > 1) { const unsigned t1 = now32(); while (now32() - t1 < (unsigned)QW1) __builtin_amdgcn_s_sleep(1); }
        if (need) {
          asm volatile("global_load_dwordx4 %0, %4, off sc1\n\t"
                       "global_load_dwordx4 %1, %4, off offset:1024 sc1\n\t"
                       "global_load_dwordx4 %2, %4, off offset:2048 sc1\n\t"
                       "global_load_dwordx4 %3, %4, off offset:3072 sc1\n\t"
                       "s_waitcnt vmcnt(0)"
                       : "+&v"(g0), "+&v"(g1), "+&v"(g2), "+&v"(g3) : "v"(qg) : "memory");
        }
      }
      unsigned* ql = reinterpret_cast<unsigned*>(lds + L_Q);
      ql[lane] = (g0[0] & 0xffffu) | (g0[2] << 16);
      ql[64 + lane] = (g1[0] & 0xffffu) | (g1[2] << 16);
      ql[128 + lane] = (g2[0] & 0xffffu) | (g2[2] << 16);
      ql[192 + lane] = (g3[0] & 0xffffu) | (g3[2] << 16);
      WAITL0();
      if (lane == 0) flag[0] = 1u;
    }
    STAMP(2);
    {
      const u32x4 e4 = {EMPTY, EMPTY, EMPTY, EMPTY};
      unsigned char* p = in2 + (size_t)me * IN2_STRIDE + lane * 16;
      st_sc1_x4(p, e4); st_sc1_x4(p + 1024, e4);
      if (lane < 16) st_sc1_x4(p + 2048, e4);
      unsigned char* q = yb_ + (size_t)b * 2048 + lane * 16;
      st_sc1_x4(q, e4);
      if (lane < 48) st_sc1_x4(q + 1024, e4);
      WAITV(0);
      if (lane == 0) st_sc1_x2(a.ws + WS_RDY + (size_t)b * 8, Tw);
    }
    if (b == MASTER) {
      const unsigned char* rp = a.ws + WS_RDY + lane * 16;
      unsigned spins = 0;
      for (;;) {
        u32x4 q0, q1;
        asm volatile("global_load_dwordx4 %0, %2, off sc1\n\tglobal_load_dwordx4 %1, %2, off offset:1024 sc1\n\ts_waitcnt vmcnt(0)"
                     : "=&v"(q0), "=&v"(q1) : "v"(rp) : "memory");
        const unsigned tl = Tw[0], th = Tw[1];
        const bool ok = (q0[0] == tl) & (q0[1] == th) & (q0[2] == tl) & (q0[3] == th) & (q1[0] == tl) & (q1[1] == th) & (q1[2] == tl) & (q1[3] == th);
        if (__all(ok)) break;
        if (dead || ++spins > SPIN_G) { dead = true; break; }
        __builtin_amdgcn_s_sleep(8);
      }
      if (lane < 32) st_sc1_x2(a.ws + WS_BC + lane * 512, Tw);
    }
    __builtin_amdgcn_s_setprio(0);
    {
      float t = 0.f;
#pragma unroll
      for (int j = 0; j < 4; ++j) { t += rbf(lo16(xx[j]) * lo16(gg[j])); t += rbf(hi16(xx[j]) * hi16(gg[j])); }
      if (lane >= 56) t = 0.f;
      t = rbf(wave_sum(t));
      const float g = rbf(1.f / (1.f + __expf(-t)));
      float yb[8];
#pragma unroll
      for (int j = 0; j < 8; ++j) yb[j] = 0.f;
      if (xs < 28) {
        { unsigned spins = 0; while ((flag[8] & flag[9] & flag[10] & flag[11]) == 0u) { __builtin_amdgcn_s_sleep(4); if (++spins > SPIN_L) break; } }
        asm volatile("" ::: "memory");
        u32x4 wf[14];
#pragma unroll
        for (int i = 0; i < 14; ++i) wf[i] = *reinterpret_cast<const u32x4*>(lds + L_W + (lane & 1) * 7168 + (r0 + i) * 16);
        const float gv = colsum16(wf, xv, red, lane);
        const float r = rbf(gv);
        const float up = dpp<ROR8>(r);
        const float tt = tanhf(0.7978845608028654f * (r + 0.044715f * r * r * r));
        const float hv = rbf(rbf(0.5f * r * (1.f + tt)) * up);
#pragma unroll
        for (int u = 0; u < 8; ++u) {
          const float hu = __builtin_bit_cast(float, __builtin_amdgcn_readlane(__builtin_bit_cast(int, hv), u));
          const u32x4 wd = *reinterpret_cast<const u32x4*>(lds + L_WD + u * 896 + lc * 16);
#pragma unroll
          for (int j = 0; j < 4; ++j) { yb[2 * j] = fmaf(hu, lo16(wd[j]), yb[2 * j]); yb[2 * j + 1] = fmaf(hu, hi16(wd[j]), yb[2 * j + 1]); }
        }
      }
      const float omg = rbf(1.f - g);
      float* yl = reinterpret_cast<float*>(lds + L_YB);
      if (lane < 56) {
        *reinterpret_cast<f32x4*>(yl + lane * 8) = f32x4{yb[0] * omg, yb[1] * omg, yb[2] * omg, yb[3] * omg};
        *reinterpret_cast<f32x4*>(yl + lane * 8 + 4) = f32x4{yb[4] * omg, yb[5] * omg, yb[6] * omg, yb[7] * omg};
      }
      if (lane == 0) yl[448] = g;
      WAITL0();
      if (lane == 0) flag[12] = 1u;
    }
    STAMP(3);
    __builtin_amdgcn_s_setprio(3);
    STAMP(4);
    if (b < NC3) {
      { unsigned spins = 0; while (flag[3] < 4u) { __builtin_amdgcn_s_sleep(2); if (++spins > SPIN_L) break; } }
      STAMP(6);
      const unsigned char* p = yb_ + (size_t)lane * 2048 + b * 16;
      constexpr int RS = 64 * 2048;
      const unsigned char* p1 = p + RS; const unsigned char* p2 = p + 2 * RS; const unsigned char* p3 = p + 3 * RS;
      u32x4 y0 = {EMPTY, EMPTY, EMPTY, EMPTY}, y1 = y0, y2 = y0, y3 = y0;
      unsigned spins = 0;
      for (;;) {
        auto bad = [&](const u32x4& y) { return (y[0] == EMPTY) | (y[1] == EMPTY) | (y[2] == EMPTY) | (y[3] == EMPTY); };
        const bool need = bad(y0) | bad(y1) | bad(y2) | bad(y3);
        if (!__any(need)) break;
        if (dead || ++spins > SPIN_G) break;
        if (need) {
          asm volatile("global_load_dwordx4 %0, %4, off sc1\n\t"
                       "global_load_dwordx4 %1, %5, off sc1\n\t"
                       "global_load_dwordx4 %2, %6, off sc1\n\t"
                       "global_load_dwordx4 %3, %7, off sc1\n\t"
                       "s_waitcnt vmcnt(0)"
                       : "+&v"(y0), "+&v"(y1), "+&v"(y2), "+&v"(y3) : "v"(p), "v"(p1), "v"(p2), "v"(p3) : "memory");
        }
      }
      STAMP(7);
      float v[4];
#pragma unroll
      for (int j = 0; j < 4; ++j) v[j] = wave_sum((__uint_as_float(y0[j]) + __uint_as_float(y1[j])) + (__uint_as_float(y2[j]) + __uint_as_float(y3[j])));
      if (lane == 0) {
        *reinterpret_cast<u32x2*>(a.out + b * 4) = u32x2{cvtpk(v[0], v[1]), cvtpk(v[2], v[3])};
        if (b == 0) st_sc1_x2(a.ws + WS_E, Tw);
      }
      STAMP(8);
    }
    DUMP(0, 9);
    return;
  }

  SW c;
  const int nt = (s & 1) ? NT - BIAS : NT + BIAS;
  const int tok0 = (s >> 1) * (2 * TOK_PER_WG) + (s & 1) * (NT + BIAS) * 64;
  const size_t base = ((size_t)h * SK + (size_t)tok0) * DH * 2;
  c.rk = __builtin_amdgcn_make_buffer_rsrc((void*)((const char*)a.kc + base), 0, nt * 64 * DH * 2, 0x00020000);
  c.rv = __builtin_amdgcn_make_buffer_rsrc((void*)((const char*)a.vc + base), 0, nt * 64 * DH * 2, 0x00020000);
  const int o0 = (2 * h + (s >> 4)) * 256 + (s & 15) * 16;
  const __amdgpu_buffer_rsrc_t rws = __builtin_amdgcn_make_buffer_rsrc((void*)a.ws, 0, 65536, 0x00020000);
  const unsigned hi = lane >> 5, pc = lane & 31;
#pragma unroll
  for (int j = 0; j < 8; ++j) c.vk[j] = (hi * 512u + ((pc ^ hi) << 4)) ^ (unsigned)(j << 5);
#pragma unroll
  for (int j = 0; j < 4; ++j) c.vv[j] = (hi * 512u + ((pc ^ (hi << 1)) << 4)) ^ (unsigned)(j << 6);
  c.m0base = ldsb + L_RING + wave * 32768u;
  const unsigned cc = lane & 15, g = lane >> 4;
#pragma unroll
  for (int q = 0; q < 4; ++q) c.ka[q] = c.m0base + cc * 512u + ((g ^ (cc & 3)) << 4) + ((q ^ (cc >> 2)) << 6);
  {
    const unsigned qq = cc >> 2, p = cc & 3, t = 4 * g + qq;
#pragma unroll
    for (int e = 0; e < 8; ++e) c.va[e] = c.m0base + t * 512u + ((e ^ (t & 7)) << 5) + p * 8u;
  }
  const int SB = 16 + 12 * wave;
  STAMP0(SB, SB + 12);
  STAMP0(9 + wave, 10 + wave);
  BAR();
  const unsigned vl16 = lane * 16u;
  auto FFN_DMA = [&]() {
    const CAS Args* ka = kargs();
    int wv = wave, uu = u0; unsigned lb = ldsb;
    asm volatile("" : "+s"(wv), "+s"(uu), "+s"(lb));
    const __amdgpu_buffer_rsrc_t rwg = __builtin_amdgcn_make_buffer_rsrc((void*)(ka->wg + uu), 0, (D * F - uu) * 2, 0x00020000);
    const __amdgpu_buffer_rsrc_t rwu = __builtin_amdgcn_make_buffer_rsrc((void*)(ka->wu + uu), 0, (D * F - uu) * 2, 0x00020000);
    const __amdgpu_buffer_rsrc_t rwd = __builtin_amdgcn_make_buffer_rsrc((void*)(ka->wd + (size_t)uu * D), 0, 8 * D * 2, 0x00020000);
    const unsigned vrow = lane * (unsigned)(F * 2);
#pragma unroll
    for (int i = 0; i < 4; ++i) {
      const unsigned k = wv + 4 * i;
      if (k < 7) DMA1F(lb + L_W + k * 1024u, rwg, k * (64u * F * 2), vrow);
      else if (k < 14) DMA1F(lb + L_W + k * 1024u, rwu, (k - 7) * (64u * F * 2), vrow);
    }
#pragma unroll
    for (int i = 0; i < 2; ++i) {
      const unsigned k = ((wv + 2) & 3) + 4 * i;
      if (k < 7) DMA1(lb + L_WD + k * 1024u, rwd, k * 1024u, vl16);
    }
  };
  unsigned sob = wave * 8192u;
  const unsigned tbeg = now32();
  fill<0>(c, sob);
  STAMP(SB);
  { unsigned spins = 0; while (flag[0] == 0u) { __builtin_amdgcn_s_sleep(1); if (++spins > SPIN_L) break; } }
  asm volatile("" ::: "memory");
  STAMP(SB + 1);
  asm volatile("" : "+s"(E));
  const u64 T = next_tag(E);
#pragma unroll
  for (int st = 0; st < 8; ++st) {
    const u32x4 qr = *reinterpret_cast<const u32x4*>(lds + L_Q + (cc & 1) * 512 + 64 * st + 16 * g);
    const u32x4 z = {0u, 0u, 0u, 0u};
    c.qf[st] = __builtin_bit_cast(bf16x8, cc < 2 ? qr : z);
  }
  c.m = -INFINITY; c.l = 0.f;
#pragma unroll
  for (int cb = 0; cb < 16; ++cb) c.acc[cb] = f32x4{0.f, 0.f, 0.f, 0.f};
  s16x4 P = {0, 0, 0, 0};
  steps_first<0>(c, sob, P);
  sob += 2 * NW * 8192u;
  const int itend = nt - ((BIAS & 1) ? 1 : 2);
  for (int it = 2; it < itend; it += 2) {
    steps_mid<0>(c, sob, P);
    sob += 2 * NW * 8192u;
    if (it == FFN_IT && xs < 28) FFN_DMA();
    if (it == FFN_IT + 2) { if (lane == 0) flag[8 + wave] = 1u; }
    if (it == WO_IT) {
      { unsigned spins = 0; while (flag[12] == 0u) { __builtin_amdgcn_s_sleep(1); if (++spins > SPIN_L) break; } }
      int wv = wave, oo = o0; unsigned lb = ldsb;
      asm volatile("" : "+s"(wv), "+s"(oo), "+s"(lb));
      const __amdgpu_buffer_rsrc_t rwo = __builtin_amdgcn_make_buffer_rsrc((void*)(kargs()->wo + (size_t)oo * D), 0, 16 * D * 2, 0x00020000);
#pragma unroll
      for (int i = 0; i < 4; ++i) {
        const unsigned k = wv + 4 * i;
        if (k < 14) DMA1(lb + L_W + k * 1024u, rwo, k * 1024u, vl16);
      }
    }
    if (it == 20 + 2 * wave) { if (lane == 0) DMAC4(ldsb + L_BC, rws, (unsigned)WS_BC + (b & 31) * 512u, 0u * lane); }
  }
  steps_last<0>(c, sob, P);
  STAMP(SB + 9);
  const float lsum = xrow16_sum(c.l);
  {
    float* mrg = reinterpret_cast<float*>(lds + L_RING + wave * 32768u);
    if (cc < 2) {
#pragma unroll
      for (int cb = 0; cb < 16; ++cb) *reinterpret_cast<f32x4*>(mrg + cc * 260 + 4 + 16 * cb + 4 * g) = c.acc[cb];
      if (g == 0) { mrg[cc * 260] = c.m; mrg[cc * 260 + 1] = lsum; }
    }
  }
  WAITL0();
  if (lane == 0) __hip_atomic_fetch_add((LAS unsigned*)(flag + 2), 1u, __ATOMIC_RELAXED, __HIP_MEMORY_SCOPE_WORKGROUP);
  {
    unsigned spins = 0;
    while (bc[0] != (unsigned)T || bc[1] != (unsigned)(T >> 32)) {
      if (lane == 0) DMAC4(ldsb + L_BC, rws, (unsigned)WS_BC + (b & 31) * 512u, 0u * lane);
      WAITV(0);
      if (++spins > SPIN_G) break;
    }
  }
  { unsigned spins = 0; while (flag[2] < 4u) { if (++spins > SPIN_L) break; } }
  asm volatile("" ::: "memory");
  if (wave < 3) {
    const int r = wave < 2 ? wave : (lane >> 4) & 1;
    float mw[NW], M = -INFINITY;
#pragma unroll
    for (int w = 0; w < NW; ++w) { mw[w] = reinterpret_cast<const float*>(lds + L_RING + w * 32768u)[r * 260]; M = fmaxf(M, mw[w]); }
    float wt[NW];
#pragma unroll
    for (int w = 0; w < NW; ++w) wt[w] = __builtin_amdgcn_exp2f((mw[w] - M) * C2);
    if (wave < 2) {
      const int d4 = lane * 4;
      f32x4 o = {0.f, 0.f, 0.f, 0.f};
#pragma unroll
      for (int w = 0; w < NW; ++w) o += wt[w] * *reinterpret_cast<const f32x4*>(reinterpret_cast<const float*>(lds + L_RING + w * 32768u) + r * 260 + 4 + d4);
      const int cons = h * 32 + r * 16 + (d4 >> 4);
      st_sc1_x4(in2 + (size_t)cons * IN2_STRIDE + s * 64 + (d4 & 15) * 4, u32x4{guard(o[0]), guard(o[1]), guard(o[2]), guard(o[3])});
    } else if (lane < 32) {
      float L = 0.f;
#pragma unroll
      for (int w = 0; w < NW; ++w) L = fmaf(wt[w], reinterpret_cast<const float*>(lds + L_RING + w * 32768u)[r * 260 + 1], L);
      const int cons = h * 32 + lane;
      st_sc1_x2(in2 + (size_t)cons * IN2_STRIDE + 2048 + (s & 15) * 16 + (s >> 4) * 8, u32x2{guard(M), guard(L)});
    }
  }
  STAMP(SB + 10);
  {
    const int lc = lane < 56 ? lane : 55;
    unsigned wp2[8][2];
    {
      const unsigned* wl = reinterpret_cast<const unsigned*>(lds + L_W) + wave * 56 + lc;
#pragma unroll
      for (int p = 0; p < 8; ++p) {
        const unsigned r0 = wl[(2 * p) * 224], r1 = wl[(2 * p + 1) * 224];
        wp2[p][0] = __builtin_amdgcn_perm(r1, r0, 0x05040100u);
        wp2[p][1] = __builtin_amdgcn_perm(r1, r0, 0x07060302u);
      }
    }
    { unsigned spins = 0; while (flag[12] == 0u) { if (++spins > SPIN_L) break; } }
    const f32x2 ybv = *reinterpret_cast<const f32x2*>(lds + L_YB + (wave * 112 + lc * 2) * 4);
    const float gate = *reinterpret_cast<const float*>(lds + L_YB + 448 * 4);
    const unsigned char* ip = in2 + (size_t)me * IN2_STRIDE;
    const unsigned char* po = ip + lane * 16;
    const unsigned char* pm = ip + 2048 + (lane >> 2) * 16;
    u32x4 oa = {EMPTY, EMPTY, EMPTY, EMPTY}, ob = oa, ml = oa;
    unsigned spins = 0;
    for (;;) {
      auto bad = [&](const u32x4& y) { return (y[0] == EMPTY) | (y[1] == EMPTY) | (y[2] == EMPTY) | (y[3] == EMPTY); };
      const bool need = bad(oa) | bad(ob) | bad(ml);
      if (!__any(need)) break;
      if (++spins > SPIN_G) break;
      if (need) {
        asm volatile("global_load_dwordx4 %0, %3, off sc1\n\t"
                     "global_load_dwordx4 %1, %3, off offset:1024 sc1\n\t"
                     "global_load_dwordx4 %2, %4, off sc1\n\t"
                     "s_waitcnt vmcnt(0)"
                     : "+&v"(oa), "+&v"(ob), "+&v"(ml) : "v"(po), "v"(pm) : "memory");
      }
    }
    STAMP(9 + wave);
    const float m_a = __uint_as_float(ml[0]), l_a = __uint_as_float(ml[1]), m_b = __uint_as_float(ml[2]), l_b = __uint_as_float(ml[3]);
    const float M = wave_max(fmaxf(m_a, m_b));
    const float wa = __builtin_amdgcn_exp2f((m_a - M) * C2), wb = __builtin_amdgcn_exp2f((m_b - M) * C2);
    const float L = wave_sum(fmaf(wa, l_a, wb * l_b)) * 0.25f;
    float o4[4];
#pragma unroll
    for (int j = 0; j < 4; ++j) {
      float t = fmaf(wa, __uint_as_float(oa[j]), wb * __uint_as_float(ob[j]));
      t += dpp<ROR4>(t); t += dpp<ROR8>(t);
      o4[j] = xrow16_sum(t);
    }
    const float inv = 1.f / L;
    const unsigned p01 = cvtpk(o4[0] * inv, o4[1] * inv), p23 = cvtpk(o4[2] * inv, o4[3] * inv);
    unsigned op[8];
    op[0] = dppu<0x00>(p01); op[1] = dppu<0x00>(p23); op[2] = dppu<0x55>(p01); op[3] = dppu<0x55>(p23);
    op[4] = dppu<0xAA>(p01); op[5] = dppu<0xAA>(p23); op[6] = dppu<0xFF>(p01); op[7] = dppu<0xFF>(p23);
    float y0 = 0.f, y1 = 0.f;
#pragma unroll
    for (int p = 0; p < 8; ++p) { y0 = dot2(wp2[p][0], op[p], y0); y1 = dot2(wp2[p][1], op[p], y1); }
    y0 = fmaf(gate, y0, ybv[0]); y1 = fmaf(gate, y1, ybv[1]);
    if (lane < 56) {
      st_sc1_x2(yb_ + (size_t)b * 2048 + (wave * 112 + lane * 2) * 4, u32x2{guard(y0), guard(y1)});
    }
  }
  STAMP(SB + 11);
  if (b < NC3 && lane == 0) __hip_atomic_fetch_add((LAS unsigned*)(flag + 3), 1u, __ATOMIC_RELAXED, __HIP_MEMORY_SCOPE_WORKGROUP);
  DUMP(SB, SB + 12);
  DUMP(9 + wave, 10 + wave);
}

extern "C" void kernel_launch(void* const* d_in, const int* in_sizes, int n_in, void* d_out, int out_size, void* d_ws, size_t ws_size, hipStream_t stream) {
  Args a;
  a.x = (const u16*)d_in[0]; a.kc = (const u16*)d_in[1]; a.vc = (const u16*)d_in[2]; a.wq = (const u16*)d_in[3]; a.wo = (const u16*)d_in[4];
  a.wg = (const u16*)d_in[6]; a.wu = (const u16*)d_in[7]; a.wd = (const u16*)d_in[8]; a.gw = (const u16*)d_in[9];
  a.out = (u16*)d_out; a.ws = (unsigned char*)d_ws;
  (void)hipFuncSetAttribute((const void*)fused_kernel, hipFuncAttributeMaxDynamicSharedMemorySize, LDS_BYTES);
  fused_kernel<<<NWG, (NW + 1) * 64, LDS_BYTES, stream>>>(a);
}
```

```cpp
#include <hip/hip_runtime.h>
#include <cstdint>

using bf16x2 = __attribute__((ext_vector_type(2))) __bf16;
using bf16x8 = __attribute__((ext_vector_type(8))) __bf16;
using s16x4  = __attribute__((ext_vector_type(4))) short;
using u32x4  = __attribute__((ext_vector_type(4))) unsigned;
using u32x2  = __attribute__((ext_vector_type(2))) unsigned;
using f32x4  = __attribute__((ext_vector_type(4))) float;
using f32x2  = __attribute__((ext_vector_type(2))) float;
typedef u32x4 u32x4_a4 __attribute__((aligned(4)));
typedef unsigned long long u64;
typedef unsigned short u16;
#define LAS __attribute__((address_space(3)))
#define GAS __attribute__((address_space(1)))
#define CAS __attribute__((address_space(4)))
#define DI __device__ __forceinline__

constexpr int D = 448, HQ = 16, HKV = 8, SK = 65536, DH = 256, F = 1792;
constexpr int NSPLIT = 32;
constexpr int TOK_PER_WG = SK / NSPLIT;
constexpr int NW = 4;
constexpr int NT = TOK_PER_WG / 16 / NW;
constexpr int NP = 4 * NT;
constexpr int NWG = HKV * NSPLIT;
constexpr int NC3 = 112;
constexpr int NFF = 224;
constexpr float C2 = 0.0625f * 1.4426950408889634f;

#define NK 21
#define QFIX 1
#define MASTER 255
#define QW0 270
#define QD 40
#define QW1 30
constexpr size_t WS_E = 0;
constexpr size_t WS_RDY = 4096;
constexpr size_t WS_QG = 8192;
constexpr size_t WS_BC = 40960;
constexpr size_t WS_IN2 = 65536;
constexpr size_t IN2_STRIDE = 4096;
constexpr size_t WS_Y = WS_IN2 + 256 * IN2_STRIDE;
constexpr size_t WS_TS = WS_Y + 512 * 1024;
constexpr size_t WS_QP = 0x1C0000;
constexpr size_t WS_FP = 0x1C8000;
constexpr unsigned L_RING = 0;
constexpr unsigned L_Q = 131072;
constexpr unsigned L_RED = L_Q + 1024;
constexpr unsigned L_W = L_RED + 4096;
constexpr unsigned L_X = L_W + 14336;
constexpr unsigned L_YB = L_X + 1024;
constexpr unsigned L_FLAG = L_YB + 1856;
constexpr unsigned L_TS = L_FLAG + 128;
constexpr unsigned LDS_BYTES = L_TS + 512;
constexpr unsigned EMPTY = 0xFFFFFFFFu;
constexpr unsigned SPIN_G = 1u << 17, SPIN_L = 1u << 22;

DI unsigned cvtpk(float lo, float hi) { f32x2 v = {lo, hi}; bf16x2 b = __builtin_convertvector(v, bf16x2); return __builtin_bit_cast(unsigned, b); }
DI float rbf(float f) { return __uint_as_float(cvtpk(f, 0.f) << 16); }
DI float lo16(unsigned w) { return __uint_as_float(w << 16); }
DI float hi16(unsigned w) { return __uint_as_float(w & 0xffff0000u); }
DI float dot2(unsigned a, unsigned b, float acc) { return __builtin_amdgcn_fdot2_f32_bf16(__builtin_bit_cast(bf16x2, a), __builtin_bit_cast(bf16x2, b), acc, false); }
DI unsigned guard(float f) { unsigned u = __float_as_uint(f); return u < 0xFFFFFFFEu ? u : 0xFFFFFFFEu; }
template <int CTRL_> DI float dpp(float x) { return __builtin_bit_cast(float, __builtin_amdgcn_mov_dpp(__builtin_bit_cast(int, x), CTRL_, 0xf, 0xf, true)); }
template <int CTRL_> DI unsigned dppu(unsigned x) { return (unsigned)__builtin_amdgcn_mov_dpp((int)x, CTRL_, 0xf, 0xf, true); }
constexpr int ROR4 = 0x124, ROR8 = 0x128, ROR1 = 0x121, ROR2 = 0x122;
DI float xrow16_max(float x) {
  auto s = __builtin_amdgcn_permlane16_swap(__float_as_uint(x), __float_as_uint(x), false, false);
  x = fmaxf(__uint_as_float(s[0]), __uint_as_float(s[1]));
  auto t = __builtin_amdgcn_permlane32_swap(__float_as_uint(x), __float_as_uint(x), false, false);
  return fmaxf(__uint_as_float(t[0]), __uint_as_float(t[1]));
}
DI float xhalf_sum(float x) {
  auto t = __builtin_amdgcn_permlane32_swap(__float_as_uint(x), __float_as_uint(x), false, false);
  return __uint_as_float(t[0]) + __uint_as_float(t[1]);
}
DI float xrow16_sum(float x) {
  auto s = __builtin_amdgcn_permlane16_swap(__float_as_uint(x), __float_as_uint(x), false, false);
  x = __uint_as_float(s[0]) + __uint_as_float(s[1]);
  return xhalf_sum(x);
}
DI float wave_sum(float x) { x += dpp<ROR1>(x); x += dpp<ROR2>(x); x += dpp<ROR4>(x); x += dpp<ROR8>(x); return xrow16_sum(x); }
DI float wave_max(float x) { x = fmaxf(x, dpp<ROR1>(x)); x = fmaxf(x, dpp<ROR2>(x)); x = fmaxf(x, dpp<ROR4>(x)); x = fmaxf(x, dpp<ROR8>(x)); return xrow16_max(x); }

DI u64 next_tag(u64 e) { const u64 t = e + 1ull; return (unsigned)t == 0u ? t + 1ull : t; }

#define LDW(p) __builtin_nontemporal_load(reinterpret_cast<const u32x4*>(p))
#define WAITV_(n) asm volatile("s_waitcnt vmcnt(" #n ")" ::: "memory")
#define WAITV(n) WAITV_(n)
#define WAITL0() asm volatile("s_waitcnt lgkmcnt(0)" ::: "memory")
#define BAR() do { asm volatile("s_waitcnt lgkmcnt(0)" ::: "memory"); __builtin_amdgcn_s_barrier(); asm volatile("" ::: "memory"); } while (0)
DI void st_sc1_x4(void* p, u32x4 v) { asm volatile("global_store_dwordx4 %0, %1, off sc1\n\ts_nop 1" :: "v"(p), "v"(v) : "memory"); }
DI void st_sc1_x1(void* p, unsigned v) { asm volatile("global_store_dword %0, %1, off sc1\n\ts_nop 1" :: "v"(p), "v"(v) : "memory"); }
DI unsigned now32() { return (unsigned)__builtin_amdgcn_s_memrealtime(); }
DI void st_sc1_x2(void* p, u32x2 v) { asm volatile("global_store_dwordx2 %0, %1, off sc1\n\ts_nop 1" :: "v"(p), "v"(v) : "memory"); }
DI unsigned rfl(unsigned v) { return (unsigned)__builtin_amdgcn_readfirstlane((int)v); }

#define STAMP(i) do { } while (0)
#define STAMP0(lo, hi) do { } while (0)
#define DUMP(lo, hi) do { } while (0)

#define DMA1(m0a, rs, so, v0)                                                              \
  asm volatile("s_mov_b32 m0, %0\n\ts_nop 0\n\t"                                           \
               "buffer_load_dwordx4 %3, %1, %2 offen nt lds"                               \
               :: "s"(m0a), "s"(rs), "s"(so), "v"(v0) : "memory")
#define DMA4(m0a, rs, so, v0, v1, v2, v3)                                                  \
  asm volatile("s_mov_b32 m0, %0\n\ts_nop 0\n\t"                                           \
               "buffer_load_dwordx4 %3, %1, %2 offen nt lds\n\t"                           \
               "buffer_load_dwordx4 %4, %1, %2 offen offset:1024 nt lds\n\t"               \
               "buffer_load_dwordx4 %5, %1, %2 offen offset:2048 nt lds\n\t"               \
               "buffer_load_dwordx4 %6, %1, %2 offen offset:3072 nt lds"                   \
               :: "s"(m0a), "s"(rs), "s"(so), "v"(v0), "v"(v1), "v"(v2), "v"(v3) : "memory")

#define DMA1K(m0a, rs, so, v0, off)                                                        \
  asm volatile("s_waitcnt vmcnt(%4)\n\ts_mov_b32 m0, %0\n\ts_nop 0\n\t"                    \
               "buffer_load_dwordx4 %3, %1, %2 offen offset:%5 nt lds"                     \
               :: "s"(m0a), "s"(rs), "s"(so), "v"(v0), "n"(NK - 1), "n"(off) : "memory")

struct SW {
  unsigned ka[4], va[8];
  bf16x8 qf[8];
  f32x4 acc[16];
  float m, l;
};

DI f32x4 qk_part(const SW& c, unsigned koff) {
  bf16x8 kf[8];
#pragma unroll
  for (int s = 0; s < 8; ++s) kf[s] = *(LAS bf16x8*)(size_t)(c.ka[s & 3] + koff + (s >> 2) * 256u);
  f32x4 s0 = {0.f, 0.f, 0.f, 0.f}, s1 = {0.f, 0.f, 0.f, 0.f};
#pragma unroll
  for (int s = 0; s < 8; s += 2) {
    s0 = __builtin_amdgcn_mfma_f32_16x16x32_bf16(kf[s], c.qf[s], s0, 0, 0, 0);
    s1 = __builtin_amdgcn_mfma_f32_16x16x32_bf16(kf[s + 1], c.qf[s + 1], s1, 0, 0, 0);
  }
  return s0 + s1;
}
DI s16x4 soft_part(SW& c, const f32x4 S) {
  float mx = fmaxf(fmaxf(S[0], S[1]), fmaxf(S[2], S[3]));
  mx = xrow16_max(mx);
  if (__any(mx > c.m)) {
    const float mn = fmaxf(c.m, mx);
    const float alpha = __builtin_amdgcn_exp2f((c.m - mn) * C2);
    c.m = mn; c.l *= alpha;
#pragma unroll
    for (int cb = 0; cb < 16; ++cb) c.acc[cb] *= alpha;
  }
  const float mc = c.m * C2;
  const float p0 = __builtin_amdgcn_exp2f(fmaf(S[0], C2, -mc)), p1 = __builtin_amdgcn_exp2f(fmaf(S[1], C2, -mc));
  const float p2 = __builtin_amdgcn_exp2f(fmaf(S[2], C2, -mc)), p3 = __builtin_amdgcn_exp2f(fmaf(S[3], C2, -mc));
  c.l += (p0 + p1) + (p2 + p3);
  u32x2 pk = {cvtpk(p0, p1), cvtpk(p2, p3)};
  return __builtin_bit_cast(s16x4, pk);
}
DI void pv_part(SW& c, const s16x4 P, unsigned voff) {
  s16x4 vf[16];
#pragma unroll
  for (int cb = 0; cb < 16; ++cb) vf[cb] = __builtin_amdgcn_ds_read_tr16_b64_v4i16((LAS s16x4*)(size_t)(c.va[cb & 7] + voff + (cb >> 3) * 256u));
#pragma unroll
  for (int cb = 0; cb < 16; ++cb) c.acc[cb] = __builtin_amdgcn_mfma_f32_16x16x16bf16_1k(vf[cb], P, c.acc[cb], 0, 0, 0);
}

DI void wait_flag(volatile LAS unsigned* f, unsigned need) {
  unsigned spins = 0;
  while (*f < need) { __builtin_amdgcn_s_sleep(1); if (++spins > SPIN_L) break; }
  asm volatile("" ::: "memory");
}
template <int SLOT> DI void tile(SW& c, unsigned t, volatile LAS unsigned* flag, int wave) {
  wait_flag(flag + 14, 16u * t + 2u * wave + 2u);
  const f32x4 S = qk_part(c, SLOT * 8192u);
  const s16x4 P = soft_part(c, S);
  WAITL0();
  if ((threadIdx.x & 63) == 0) flag[4 + wave] = t + 1u;
  wait_flag(flag + 14, 16u * t + 2u * wave + 10u);
  pv_part(c, P, 16384u + SLOT * 8192u);
  WAITL0();
  if ((threadIdx.x & 63) == 0) flag[8 + wave] = t + 1u;
}

struct Args {
  const u16* x; const u16* kc; const u16* vc; const u16* wq; const u16* wo;
  const u16* wg; const u16* wu; const u16* wd; const u16* gw;
  u16* out; unsigned char* ws;
};

DI const CAS Args* kargs() {
  const CAS Args* p = (const CAS Args*)__builtin_amdgcn_kernarg_segment_ptr();
  asm volatile("" : "+s"(p));
  return p;
}

template <int NR> DI void colacc(const u32x4 (&w)[NR], const float (&xv)[NR], float* red, int lane) {
  float acc[8];
#pragma unroll
  for (int j = 0; j < 8; ++j) acc[j] = 0.f;
#pragma unroll
  for (int i = 0; i < NR; ++i)
#pragma unroll
    for (int j = 0; j < 4; ++j) { acc[2 * j] = fmaf(xv[i], lo16(w[i][j]), acc[2 * j]); acc[2 * j + 1] = fmaf(xv[i], hi16(w[i][j]), acc[2 * j + 1]); }
  *reinterpret_cast<f32x4*>(red + lane * 8) = f32x4{acc[0], acc[1], acc[2], acc[3]};
  *reinterpret_cast<f32x4*>(red + lane * 8 + 4) = f32x4{acc[4], acc[5], acc[6], acc[7]};
}
DI float red8(const float* red, int base, int stride) {
  float t = 0.f;
#pragma unroll
  for (int i = 0; i < 8; ++i) t += red[base + i * stride];
  return t;
}

__global__ void __launch_bounds__(384, 1) fused_kernel(Args a) {
  extern __shared__ __attribute__((aligned(16))) unsigned char lds[];
  const int b = blockIdx.x;
  const int h = (b >> 3) & 7, s = (b & 7) + 8 * (b >> 6);
  const int xh = b & 7, xs = b >> 3;
  const int me = h * 32 + s;
  const int tid = threadIdx.x, lane = tid & 63, wave = __builtin_amdgcn_readfirstlane(tid >> 6);
  volatile LAS unsigned* flag = (volatile LAS unsigned*)(size_t)((unsigned)(size_t)lds + L_FLAG);
  unsigned char* in2 = a.ws + WS_IN2;
  unsigned char* yb_ = a.ws + WS_Y;

  u64 E = *(const CAS u64*)(a.ws + WS_E);
  const unsigned ldsb = (unsigned)(size_t)lds;

  if (wave == NW) {
    __builtin_amdgcn_s_setprio(3);
    const unsigned tstart = now32();
    STAMP0(0, 9);
    STAMP(0);
    const int lc = lane < 56 ? lane : 55;
    const unsigned Ls = (3u - (unsigned)((size_t)a.wq >> 7)) & 7u;
    const bool wide = (unsigned)(xs >> 2) == Ls;
    const int r0 = wide ? 112 * (xs & 3) + 14 * (lane >> 3) : 14 * (lane >> 1);
    u32x4 wqr[14], xx, gg, xa, xb;
    {
      const int col = wide ? (xs >> 2) * 64 + (lane & 7) * 8 : xs * 16 + (lane & 1) * 8;
      const u16* wp = a.wq + (size_t)r0 * 4096 + (xh * 512 + col);
#pragma unroll
      for (int i = 0; i < 14; ++i) wqr[i] = LDW(wp + (size_t)i * 4096);
      xa = *reinterpret_cast<const u32x4_a4*>(a.x + r0);
      xb = *reinterpret_cast<const u32x4_a4*>(a.x + r0 + 6);
      xx = *reinterpret_cast<const u32x4*>(a.x + lc * 8); gg = *reinterpret_cast<const u32x4*>(a.gw + lc * 8);
    }
    if (lane < 32) flag[lane] = 0u;
    BAR();
    float xv[14];
#pragma unroll
    for (int i = 0; i < 14; ++i) asm volatile("" : "+v"(wqr[i]));
    asm volatile("" : "+v"(xa), "+v"(xb));
#pragma unroll
    for (int i = 0; i < 4; ++i) { xv[2 * i] = lo16(xa[i]); xv[2 * i + 1] = hi16(xa[i]); }
#pragma unroll
    for (int i = 0; i < 3; ++i) { xv[8 + 2 * i] = lo16(xb[1 + i]); xv[9 + 2 * i] = hi16(xb[1 + i]); }
    float* red = reinterpret_cast<float*>(lds + L_RED);
    asm volatile("" : "+s"(E));
    const u64 T = next_tag(E);
    const u32x2 Tw = {(unsigned)T, (unsigned)(T >> 32)};
    {
      colacc<14>(wqr, xv, red, lane);
      WAITL0();
      float qv = red8(red, wide ? lane : (lane >> 4) * 128 + (lane & 15), wide ? 64 : 16);
      if (wide) {
        st_sc1_x2(a.ws + WS_QP + (size_t)((xh * 4 + (xs & 3)) * 64 + lane) * 8, u32x2{__float_as_uint(qv), Tw[0]});
      } else {
        qv = xrow16_sum(qv);
        const u64 gr = (T << 16) | (u64)(cvtpk(qv, 0.f) & 0xffffu);
        if (lane < 16) st_sc1_x2(a.ws + WS_QG + (size_t)(xh * 512 + xs * 16 + lane) * 8, u32x2{(unsigned)gr, (unsigned)(gr >> 32)});
      }
    }
    if (lane < 56) *reinterpret_cast<u32x4*>(lds + L_X + lane * 16) = xx;
    STAMP(1);
    bool dead = false;
    {
      const unsigned char* qg = a.ws + WS_QG + (size_t)h * 4096 + lane * 16;
      const unsigned char* qp = a.ws + WS_QP + (size_t)h * 2048 + lane * 16;
      const unsigned tlo = (unsigned)(T << 16), thi = (unsigned)(T >> 16);
      const unsigned mine = (unsigned)(lane >> 5);
      auto okg = [&](const u32x4& g, unsigned j) { return (((g[0] & 0xffff0000u) == tlo) & (g[1] == thi) & ((g[2] & 0xffff0000u) == tlo) & (g[3] == thi)) | (2 * j + mine == Ls); };
      auto okp = [&](const u32x4& g) { return ((g[1] == Tw[0]) & (g[3] == Tw[0])) | (Ls > 7u); };
      u32x4 g0, g1, g2, g3, p0, p1, e0, e1, e2, e3, f0, f1;
      while (now32() - tstart < (unsigned)QW0) __builtin_amdgcn_s_sleep(2);
#define QLOADS(a0, a1, a2, a3, b0, b1, tail)                                  \
      asm volatile("global_load_dwordx4 %0, %6, off sc1\n\t"                  \
                   "global_load_dwordx4 %1, %6, off offset:1024 sc1\n\t"      \
                   "global_load_dwordx4 %2, %6, off offset:2048 sc1\n\t"      \
                   "global_load_dwordx4 %3, %6, off offset:3072 sc1\n\t"      \
                   "global_load_dwordx4 %4, %7, off sc1\n\t"                  \
                   "global_load_dwordx4 %5, %7, off offset:1024 sc1" tail     \
                   : "=&v"(a0), "=&v"(a1), "=&v"(a2), "=&v"(a3), "=&v"(b0), "=&v"(b1) : "v"(qg), "v"(qp) : "memory")
      QLOADS(g0, g1, g2, g3, p0, p1, "");
      while (now32() - tstart < (unsigned)(QW0 + QD)) __builtin_amdgcn_s_sleep(1);
      QLOADS(e0, e1, e2, e3, f0, f1, "\n\ts_waitcnt vmcnt(6)");
      asm volatile("" : "+v"(g0), "+v"(g1), "+v"(g2), "+v"(g3), "+v"(p0), "+v"(p1));
      if (__any(!(okg(g0, 0) & okg(g1, 1) & okg(g2, 2) & okg(g3, 3) & okp(p0) & okp(p1)))) {
        WAITV(0);
        asm volatile("" : "+v"(e0), "+v"(e1), "+v"(e2), "+v"(e3), "+v"(f0), "+v"(f1));
        if (!okg(g0, 0)) g0 = e0;
        if (!okg(g1, 1)) g1 = e1;
        if (!okg(g2, 2)) g2 = e2;
        if (!okg(g3, 3)) g3 = e3;
        if (!okp(p0)) p0 = f0;
        if (!okp(p1)) p1 = f1;
        unsigned spins = 0;
        for (;;) {
          const bool need = !(okg(g0, 0) & okg(g1, 1) & okg(g2, 2) & okg(g3, 3) & okp(p0) & okp(p1));
          if (!__any(need)) break;
          if (++spins > SPIN_G) { dead = true; break; }
          if (spins > 1) { const unsigned t1 = now32(); while (now32() - t1 < (unsigned)QW1) __builtin_amdgcn_s_sleep(1); }
          if (need) {
            asm volatile("global_load_dwordx4 %0, %6, off sc1\n\t"
                         "global_load_dwordx4 %1, %6, off offset:1024 sc1\n\t"
                         "global_load_dwordx4 %2, %6, off offset:2048 sc1\n\t"
                         "global_load_dwordx4 %3, %6, off offset:3072 sc1\n\t"
                         "global_load_dwordx4 %4, %7, off sc1\n\t"
                         "global_load_dwordx4 %5, %7, off offset:1024 sc1\n\t"
                         "s_waitcnt vmcnt(0)"
                         : "+&v"(g0), "+&v"(g1), "+&v"(g2), "+&v"(g3), "+&v"(p0), "+&v"(p1) : "v"(qg), "v"(qp) : "memory");
          }
        }
      }
      unsigned* ql = reinterpret_cast<unsigned*>(lds + L_Q);
      ql[lane] = (g0[0] & 0xffffu) | (g0[2] << 16);
      ql[64 + lane] = (g1[0] & 0xffffu) | (g1[2] << 16);
      ql[128 + lane] = (g2[0] & 0xffffu) | (g2[2] << 16);
      ql[192 + lane] = (g3[0] & 0xffffu) | (g3[2] << 16);
      {
        const float qa = xhalf_sum(__uint_as_float(p0[0]) + __uint_as_float(p1[0]));
        const float qb = xhalf_sum(__uint_as_float(p0[2]) + __uint_as_float(p1[2]));
        if (lane < 32) ql[Ls * 32 + lane] = cvtpk(qa, qb);
      }
      WAITL0();
      if (lane == 0) flag[0] = 1u;
      WAITV(0);
      asm volatile("" :: "v"(e0), "v"(e1), "v"(e2), "v"(e3), "v"(f0), "v"(f1));
    }
    STAMP(2);
    const CAS Args* ka = kargs();
    u32x4 wgr[7], wur[7], wdr[8];
    const int lg = b >> 3, ks = b & 7;
    if (b < NFF) {
      const size_t o = (size_t)(56 * ks + (lane >> 3)) * F + lg * 64 + (lane & 7) * 8;
#pragma unroll
      for (int i = 0; i < 7; ++i) { wgr[i] = LDW(ka->wg + o + (size_t)i * 8 * F); wur[i] = LDW(ka->wu + o + (size_t)i * 8 * F); }
#pragma unroll
      for (int u = 0; u < 8; ++u) wdr[u] = LDW(ka->wd + (size_t)(b * 8 + u) * D + lc * 8);
    }
    {
      const u32x4 e4 = {EMPTY, EMPTY, EMPTY, EMPTY};
      unsigned char* p = in2 + (size_t)me * IN2_STRIDE + lane * 16;
      st_sc1_x4(p, e4); st_sc1_x4(p + 1024, e4);
      if (lane < 16) st_sc1_x4(p + 2048, e4);
      unsigned char* q = yb_ + (size_t)b * 2048 + lane * 16;
      st_sc1_x4(q, e4);
      if (lane < 48) st_sc1_x4(q + 1024, e4);
    }
    {
      const int o0 = (2 * h + (s >> 4)) * 256 + (s & 15) * 16;
      const __amdgpu_buffer_rsrc_t rwo = __builtin_amdgcn_make_buffer_rsrc((void*)(ka->wo + (size_t)o0 * D), 0, 16 * D * 2, 0x00020000);
      const unsigned vl16 = lane * 16u;
#pragma unroll
      for (int k = 0; k < 14; ++k) DMA1(ldsb + L_W + k * 1024u, rwo, k * 1024u, vl16);
    }
    WAITV(0);
    if (lane == 0) st_sc1_x2(a.ws + WS_RDY + (size_t)b * 8, Tw);
    __builtin_amdgcn_s_setprio(0);
    {
      float t = 0.f;
#pragma unroll
      for (int j = 0; j < 4; ++j) { t += rbf(lo16(xx[j]) * lo16(gg[j])); t += rbf(hi16(xx[j]) * hi16(gg[j])); }
      if (lane >= 56) t = 0.f;
      t = rbf(wave_sum(t));
      const float g = rbf(1.f / (1.f + __expf(-t)));
      float yb[8];
#pragma unroll
      for (int j = 0; j < 8; ++j) yb[j] = 0.f;
      if (b < NFF) {
        float xr[7];
#pragma unroll
        for (int i = 0; i < 7; ++i) xr[i] = lo16(reinterpret_cast<const u16*>(lds + L_X)[56 * ks + 8 * i + (lane >> 3)]);
        colacc<7>(wgr, xr, red, lane);
        colacc<7>(wur, xr, red + 512, lane);
        WAITL0();
        const float pg = red8(red, lane, 64), pu = red8(red + 512, lane, 64);
        st_sc1_x4(a.ws + WS_FP + (size_t)(b * 64 + lane) * 16, u32x4{__float_as_uint(pg), Tw[0], __float_as_uint(pu), Tw[0]});
        const unsigned char* fp = a.ws + WS_FP + (size_t)((lg * 8 + (lane >> 3)) * 64 + ks * 8 + (lane & 7)) * 16;
        u32x4 f = {0u, ~Tw[0], 0u, ~Tw[0]};
        unsigned spins = 0;
        for (;;) {
          const bool need = (f[1] != Tw[0]) | (f[3] != Tw[0]);
          if (!__any(need)) break;
          if (dead || ++spins > SPIN_G) { dead = true; break; }
          { const unsigned t1 = now32(); while (now32() - t1 < 60u) __builtin_amdgcn_s_sleep(2); }
          if (need) asm volatile("global_load_dwordx4 %0, %1, off sc1\n\ts_waitcnt vmcnt(0)" : "+&v"(f) : "v"(fp) : "memory");
        }
        float gs = __uint_as_float(f[0]), us = __uint_as_float(f[2]);
        gs += dpp<ROR8>(gs); us += dpp<ROR8>(us);
        const float r = rbf(xrow16_sum(gs)), up = rbf(xrow16_sum(us));
        const float tt = tanhf(0.7978845608028654f * (r + 0.044715f * r * r * r));
        const float hv = rbf(rbf(0.5f * r * (1.f + tt)) * up);
#pragma unroll
        for (int u = 0; u < 8; ++u) {
          const float hu = __builtin_bit_cast(float, __builtin_amdgcn_readlane(__builtin_bit_cast(int, hv), u));
#pragma unroll
          for (int j = 0; j < 4; ++j) { yb[2 * j] = fmaf(hu, lo16(wdr[u][j]), yb[2 * j]); yb[2 * j + 1] = fmaf(hu, hi16(wdr[u][j]), yb[2 * j + 1]); }
        }
      }
      const float omg = rbf(1.f - g);
      float* yl = reinterpret_cast<float*>(lds + L_YB);
      if (lane < 56) {
        *reinterpret_cast<f32x4*>(yl + lane * 8) = f32x4{yb[0] * omg, yb[1] * omg, yb[2] * omg, yb[3] * omg};
        *reinterpret_cast<f32x4*>(yl + lane * 8 + 4) = f32x4{yb[4] * omg, yb[5] * omg, yb[6] * omg, yb[7] * omg};
      }
      if (lane == 0) yl[448] = g;
      WAITL0();
      if (lane == 0) flag[12] = 1u;
    }
    STAMP(3);
    if (b == MASTER) {
      const unsigned char* rp = a.ws + WS_RDY + lane * 16;
      unsigned spins = 0;
      for (;;) {
        u32x4 q0, q1;
        asm volatile("global_load_dwordx4 %0, %2, off sc1\n\tglobal_load_dwordx4 %1, %2, off offset:1024 sc1\n\ts_waitcnt vmcnt(0)"
                     : "=&v"(q0), "=&v"(q1) : "v"(rp) : "memory");
        const unsigned tl = Tw[0], th = Tw[1];
        const bool ok = (q0[0] == tl) & (q0[1] == th) & (q0[2] == tl) & (q0[3] == th) & (q1[0] == tl) & (q1[1] == th) & (q1[2] == tl) & (q1[3] == th);
        if (__all(ok)) break;
        if (dead || ++spins > SPIN_G) { dead = true; break; }
        __builtin_amdgcn_s_sleep(8);
      }
      if (lane < 32) st_sc1_x2(a.ws + WS_BC + lane * 512, Tw);
    }
    {
      const unsigned char* pp = a.ws + WS_BC + (b & 31) * 512;
      unsigned spins = 0;
      for (;;) {
        u32x2 ent;
        asm volatile("global_load_dwordx2 %0, %1, off sc1\n\ts_waitcnt vmcnt(0)" : "=&v"(ent) : "v"(pp) : "memory");
        if (rfl(ent[0]) == Tw[0] && rfl(ent[1]) == Tw[1]) break;
        if (dead || ++spins > SPIN_G) break;
        { const unsigned t1 = now32(); while (now32() - t1 < 200u) __builtin_amdgcn_s_sleep(8); }
      }
      if (lane == 0) flag[13] = 1u;
    }
    __builtin_amdgcn_s_setprio(3);
    STAMP(4);
    if (b < NC3) {
      { unsigned spins = 0; while (flag[3] < 4u) { __builtin_amdgcn_s_sleep(2); if (++spins > SPIN_L) break; } }
      STAMP(6);
      const unsigned char* p = yb_ + (size_t)lane * 2048 + b * 16;
      constexpr int RS = 64 * 2048;
      const unsigned char* p1 = p + RS; const unsigned char* p2 = p + 2 * RS; const unsigned char* p3 = p + 3 * RS;
      u32x4 y0 = {EMPTY, EMPTY, EMPTY, EMPTY}, y1 = y0, y2 = y0, y3 = y0;
      unsigned spins = 0;
      for (;;) {
        auto bad = [&](const u32x4& y) { return (y[0] == EMPTY) | (y[1] == EMPTY) | (y[2] == EMPTY) | (y[3] == EMPTY); };
        const bool need = bad(y0) | bad(y1) | bad(y2) | bad(y3);
        if (!__any(need)) break;
        if (dead || ++spins > SPIN_G) break;
        if (need) {
          asm volatile("global_load_dwordx4 %0, %4, off sc1\n\t"
                       "global_load_dwordx4 %1, %5, off sc1\n\t"
                       "global_load_dwordx4 %2, %6, off sc1\n\t"
                       "global_load_dwordx4 %3, %7, off sc1\n\t"
                       "s_waitcnt vmcnt(0)"
                       : "+&v"(y0), "+&v"(y1), "+&v"(y2), "+&v"(y3) : "v"(p), "v"(p1), "v"(p2), "v"(p3) : "memory");
        }
      }
      STAMP(7);
      float v[4];
#pragma unroll
      for (int j = 0; j < 4; ++j) v[j] = wave_sum((__uint_as_float(y0[j]) + __uint_as_float(y1[j])) + (__uint_as_float(y2[j]) + __uint_as_float(y3[j])));
      if (lane == 0) {
        *reinterpret_cast<u32x2*>(a.out + b * 4) = u32x2{cvtpk(v[0], v[1]), cvtpk(v[2], v[3])};
        if (b == 0) st_sc1_x2(a.ws + WS_E, Tw);
      }
      STAMP(8);
    }
    DUMP(0, 9);
    return;
  }

  const unsigned hi = lane >> 5, pc = lane & 31;
  if (wave == NW + 1) {
    __builtin_amdgcn_s_setprio(2);
    const size_t base = ((size_t)h * SK + (size_t)s * TOK_PER_WG) * DH * 2;
    const __amdgpu_buffer_rsrc_t rk = __builtin_amdgcn_make_buffer_rsrc((void*)((const char*)a.kc + base), 0, TOK_PER_WG * DH * 2, 0x00020000);
    const __amdgpu_buffer_rsrc_t rv = __builtin_amdgcn_make_buffer_rsrc((void*)((const char*)a.vc + base), 0, TOK_PER_WG * DH * 2, 0x00020000);
    unsigned vk[8], vv[4];
#pragma unroll
    for (int j = 0; j < 8; ++j) vk[j] = (hi * 512u + ((pc ^ hi) << 4)) ^ (unsigned)(j << 5);
#pragma unroll
    for (int j = 0; j < 4; ++j) vv[j] = (hi * 512u + ((pc ^ (hi << 1)) << 4)) ^ (unsigned)(j << 6);
    BAR();
    int L = 0;
    for (int t = 0; t < NT; ++t) {
      const unsigned so0 = (unsigned)t * (NW * 8192u), lo0 = ldsb + L_RING + (t & 1) * 8192u;
#pragma unroll
      for (int p = 0; p < 16; ++p) {
        const int isV = p >> 3, w = (p >> 1) & 3, half = p & 1, g = 16 * t + p;
        if (half == 0 && t >= 2) {
          if (flag[4 + 4 * isV + w] < (unsigned)(t - 1)) {
            WAITV(0);
            if (g == 32) STAMP(16);
            L = g;
            if (lane == 0) flag[14] = (unsigned)L;
            wait_flag(flag + 4 + 4 * isV + w, (unsigned)(t - 1));
          }
        }
        const unsigned so = so0 + w * 8192u + half * 4096u, lo = lo0 + w * 32768u + isV * 16384u + half * 4096u;
#pragma unroll
        for (int k = 0; k < 4; ++k) {
          if (isV) DMA1K(lo, rv, so, vv[k], k * 1024);
          else DMA1K(lo, rk, so, vk[4 * half + k], k * 1024);
          if (k == ((NK - 1) & 3)) {
            const int Ln = g - (NK - 1) / 4;
            if (Ln > L) { L = Ln; if (lane == 0) flag[14] = (unsigned)L; }
          }
        }
      }
    }
#pragma unroll
    for (int k = (NK + 2) / 4; k >= 0; --k) {
      asm volatile("s_waitcnt vmcnt(%0)" :: "n"(4 * k) : "memory");
      if (16 * NT - k > L) { L = 16 * NT - k; if (lane == 0) flag[14] = (unsigned)L; }
    }
    return;
  }

  SW c;
  const unsigned m0base = ldsb + L_RING + wave * 32768u;
  const unsigned cc = lane & 15, g = lane >> 4;
#pragma unroll
  for (int q = 0; q < 4; ++q) c.ka[q] = m0base + cc * 512u + ((g ^ (cc & 3)) << 4) + ((q ^ (cc >> 2)) << 6);
  {
    const unsigned qq = cc >> 2, p = cc & 3, t = 4 * g + qq;
#pragma unroll
    for (int e = 0; e < 8; ++e) c.va[e] = m0base + t * 512u + ((e ^ (t & 7)) << 5) + p * 8u;
  }
  const int SB = 16 + 12 * wave;
  STAMP0(SB, SB + 12);
  STAMP0(9 + wave, 10 + wave);
  BAR();
  wait_flag(flag, 1u);
  STAMP(SB + 1);
#pragma unroll
  for (int st = 0; st < 8; ++st) {
    const u32x4 qr = *reinterpret_cast<const u32x4*>(lds + L_Q + (cc & 1) * 512 + 64 * st + 16 * g);
    const u32x4 z = {0u, 0u, 0u, 0u};
    c.qf[st] = __builtin_bit_cast(bf16x8, cc < 2 ? qr : z);
  }
  c.m = -INFINITY; c.l = 0.f;
#pragma unroll
  for (int cb = 0; cb < 16; ++cb) c.acc[cb] = f32x4{0.f, 0.f, 0.f, 0.f};
  for (unsigned t = 0; t < (unsigned)NT; t += 2) {
    tile<0>(c, t, flag, wave);
    tile<1>(c, t + 1, flag, wave);
  }
  STAMP(SB + 9);
  const float lsum = xrow16_sum(c.l);
  {
    float* mrg = reinterpret_cast<float*>(lds + L_RING + wave * 32768u);
    if (cc < 2) {
#pragma unroll
      for (int cb = 0; cb < 16; ++cb) *reinterpret_cast<f32x4*>(mrg + cc * 260 + 4 + 16 * cb + 4 * g) = c.acc[cb];
      if (g == 0) { mrg[cc * 260] = c.m; mrg[cc * 260 + 1] = lsum; }
    }
  }
  WAITL0();
  if (lane == 0) __hip_atomic_fetch_add((LAS unsigned*)(flag + 2), 1u, __ATOMIC_RELAXED, __HIP_MEMORY_SCOPE_WORKGROUP);
  { unsigned spins = 0; while (flag[2] < 4u || flag[13] == 0u || flag[12] == 0u) { if (++spins > SPIN_L) break; } }
  asm volatile("" ::: "memory");
  if (wave < 3) {
    const int r = wave < 2 ? wave : (lane >> 4) & 1;
    float mw[NW], M = -INFINITY;
#pragma unroll
    for (int w = 0; w < NW; ++w) { mw[w] = reinterpret_cast<const float*>(lds + L_RING + w * 32768u)[r * 260]; M = fmaxf(M, mw[w]); }
    float wt[NW];
#pragma unroll
    for (int w = 0; w < NW; ++w) wt[w] = __builtin_amdgcn_exp2f((mw[w] - M) * C2);
    if (wave < 2) {
      const int d4 = lane * 4;
      f32x4 o = {0.f, 0.f, 0.f, 0.f};
#pragma unroll
      for (int w = 0; w < NW; ++w) o += wt[w] * *reinterpret_cast<const f32x4*>(reinterpret_cast<const float*>(lds + L_RING + w * 32768u) + r * 260 + 4 + d4);
      const int cons = h * 32 + r * 16 + (d4 >> 4);
      st_sc1_x4(in2 + (size_t)cons * IN2_STRIDE + s * 64 + (d4 & 15) * 4, u32x4{guard(o[0]), guard(o[1]), guard(o[2]), guard(o[3])});
    } else if (lane < 32) {
      float L = 0.f;
#pragma unroll
      for (int w = 0; w < NW; ++w) L = fmaf(wt[w], reinterpret_cast<const float*>(lds + L_RING + w * 32768u)[r * 260 + 1], L);
      const int cons = h * 32 + lane;
      st_sc1_x2(in2 + (size_t)cons * IN2_STRIDE + 2048 + (s & 15) * 16 + (s >> 4) * 8, u32x2{guard(M), guard(L)});
    }
  }
  STAMP(SB + 10);
  {
    const int lc = lane < 56 ? lane : 55;
    unsigned wp2[8][2];
    {
      const unsigned* wl = reinterpret_cast<const unsigned*>(lds + L_W) + wave * 56 + lc;
#pragma unroll
      for (int p = 0; p < 8; ++p) {
        const unsigned r0 = wl[(2 * p) * 224], r1 = wl[(2 * p + 1) * 224];
        wp2[p][0] = __builtin_amdgcn_perm(r1, r0, 0x05040100u);
        wp2[p][1] = __builtin_amdgcn_perm(r1, r0, 0x07060302u);
      }
    }
    const f32x2 ybv = *reinterpret_cast<const f32x2*>(lds + L_YB + (wave * 112 + lc * 2) * 4);
    const float gate = *reinterpret_cast<const float*>(lds + L_YB + 448 * 4);
    const unsigned char* ip = in2 + (size_t)me * IN2_STRIDE;
    const unsigned char* po = ip + lane * 16;
    const unsigned char* pm = ip + 2048 + (lane >> 2) * 16;
    u32x4 oa = {EMPTY, EMPTY, EMPTY, EMPTY}, ob = oa, ml = oa;
    unsigned spins = 0;
    for (;;) {
      auto bad = [&](const u32x4& y) { return (y[0] == EMPTY) | (y[1] == EMPTY) | (y[2] == EMPTY) | (y[3] == EMPTY); };
      const bool need = bad(oa) | bad(ob) | bad(ml);
      if (!__any(need)) break;
      if (++spins > SPIN_G) break;
      if (need) {
        asm volatile("global_load_dwordx4 %0, %3, off sc1\n\t"
                     "global_load_dwordx4 %1, %3, off offset:1024 sc1\n\t"
                     "global_load_dwordx4 %2, %4, off sc1\n\t"
                     "s_waitcnt vmcnt(0)"
                     : "+&v"(oa), "+&v"(ob), "+&v"(ml) : "v"(po), "v"(pm) : "memory");
      }
    }
    STAMP(9 + wave);
    const float m_a = __uint_as_float(ml[0]), l_a = __uint_as_float(ml[1]), m_b = __uint_as_float(ml[2]), l_b = __uint_as_float(ml[3]);
    const float M = wave_max(fmaxf(m_a, m_b));
    const float wa = __builtin_amdgcn_exp2f((m_a - M) * C2), wb = __builtin_amdgcn_exp2f((m_b - M) * C2);
    const float L = wave_sum(fmaf(wa, l_a, wb * l_b)) * 0.25f;
    float o4[4];
#pragma unroll
    for (int j = 0; j < 4; ++j) {
      float t = fmaf(wa, __uint_as_float(oa[j]), wb * __uint_as_float(ob[j]));
      t += dpp<ROR4>(t); t += dpp<ROR8>(t);
      o4[j] = xrow16_sum(t);
    }
    const float inv = 1.f / L;
    const unsigned p01 = cvtpk(o4[0] * inv, o4[1] * inv), p23 = cvtpk(o4[2] * inv, o4[3] * inv);
    unsigned op[8];
    op[0] = dppu<0x00>(p01); op[1] = dppu<0x00>(p23); op[2] = dppu<0x55>(p01); op[3] = dppu<0x55>(p23);
    op[4] = dppu<0xAA>(p01); op[5] = dppu<0xAA>(p23); op[6] = dppu<0xFF>(p01); op[7] = dppu<0xFF>(p23);
    float y0 = 0.f, y1 = 0.f;
#pragma unroll
    for (int p = 0; p < 8; ++p) { y0 = dot2(wp2[p][0], op[p], y0); y1 = dot2(wp2[p][1], op[p], y1); }
    y0 = fmaf(gate, y0, ybv[0]); y1 = fmaf(gate, y1, ybv[1]);
    if (lane < 56) {
      st_sc1_x2(yb_ + (size_t)b * 2048 + (wave * 112 + lane * 2) * 4, u32x2{guard(y0), guard(y1)});
    }
  }
  STAMP(SB + 11);
  if (b < NC3 && lane == 0) __hip_atomic_fetch_add((LAS unsigned*)(flag + 3), 1u, __ATOMIC_RELAXED, __HIP_MEMORY_SCOPE_WORKGROUP);
  DUMP(SB, SB + 12);
  DUMP(9 + wave, 10 + wave);
}

extern "C" void kernel_launch(void* const* d_in, const int* in_sizes, int n_in, void* d_out, int out_size, void* d_ws, size_t ws_size, hipStream_t stream) {
  Args a;
  a.x = (const u16*)d_in[0]; a.kc = (const u16*)d_in[1]; a.vc = (const u16*)d_in[2]; a.wq = (const u16*)d_in[3]; a.wo = (const u16*)d_in[4];
  a.wg = (const u16*)d_in[6]; a.wu = (const u16*)d_in[7]; a.wd = (const u16*)d_in[8]; a.gw = (const u16*)d_in[9];
  a.out = (u16*)d_out; a.ws = (unsigned char*)d_ws;
  (void)hipFuncSetAttribute((const void*)fused_kernel, hipFuncAttributeMaxDynamicSharedMemorySize, LDS_BYTES);
  fused_kernel<<<NWG, (NW + 2) * 64, LDS_BYTES, stream>>>(a);
}
```

```cpp
#include <hip/hip_runtime.h>
#include <cstdint>

using bf16x2 = __attribute__((ext_vector_type(2))) __bf16;
using bf16x8 = __attribute__((ext_vector_type(8))) __bf16;
using s16x4  = __attribute__((ext_vector_type(4))) short;
using u32x4  = __attribute__((ext_vector_type(4))) unsigned;
using u32x2  = __attribute__((ext_vector_type(2))) unsigned;
using f32x4  = __attribute__((ext_vector_type(4))) float;
using f32x2  = __attribute__((ext_vector_type(2))) float;
typedef u32x4 u32x4_a4 __attribute__((aligned(4)));
typedef unsigned long long u64;
typedef unsigned short u16;
#define LAS __attribute__((address_space(3)))
#define GAS __attribute__((address_space(1)))
#define CAS __attribute__((address_space(4)))
#define DI __device__ __forceinline__

constexpr int D = 448, HQ = 16, HKV = 8, SK = 65536, DH = 256, F = 1792;
constexpr int NSPLIT = 32;
constexpr int TOK_PER_WG = SK / NSPLIT;
constexpr int NW = 4;
constexpr int NT = TOK_PER_WG / 16 / NW;
constexpr int NP = 4 * NT;
constexpr int NWG = HKV * NSPLIT;
constexpr int NC3 = 112;
constexpr int NFF = 224;
constexpr float C2 = 0.0625f * 1.4426950408889634f;

#define NK 22
#define W0 21
#define CTL 1
#define MARG 2
#define OFFP ((NK - 1) / 4 + 1 + MARG)
#define FIXRHO 0
#define PHI 1
#define ACC 1.02f
#define RHO0 (16.f / 210.f)
#define CKR 2
#define CK0 3
#define DLY 250
#define NREP 16
#define RSTR 64
#define VT 1
#define QFIX 1
#define MASTER 255
#define QW0 270
#define QD 40
#define QW1 30
constexpr size_t WS_E = 0;
constexpr size_t WS_RDY = 4096;
constexpr size_t WS_QG = 8192;
constexpr size_t WS_BC = 40960;
constexpr size_t WS_IN2 = 65536;
constexpr size_t IN2_STRIDE = 4096;
constexpr size_t WS_Y = WS_IN2 + 256 * IN2_STRIDE;
constexpr size_t WS_TS = WS_Y + 512 * 1024;
constexpr size_t WS_QP = 0x1C0000;
constexpr size_t WS_PR = 0x200000;
constexpr size_t WS_FP = 0x1C8000;
constexpr unsigned L_RING = 0;
constexpr unsigned L_Q = 131072;
constexpr unsigned L_RED = L_Q + 1024;
constexpr unsigned L_W = L_RED + 4096;
constexpr unsigned L_X = L_W + 14336;
constexpr unsigned L_YB = L_X + 1024;
constexpr unsigned L_FLAG = L_YB + 1856;
constexpr unsigned L_TS = L_FLAG + 128;
constexpr unsigned LDS_BYTES = L_TS + 512;
constexpr unsigned EMPTY = 0xFFFFFFFFu;
constexpr unsigned SPIN_G = 1u << 17, SPIN_L = 1u << 22;

DI unsigned cvtpk(float lo, float hi) { f32x2 v = {lo, hi}; bf16x2 b = __builtin_convertvector(v, bf16x2); return __builtin_bit_cast(unsigned, b); }
DI float rbf(float f) { return __uint_as_float(cvtpk(f, 0.f) << 16); }
DI float lo16(unsigned w) { return __uint_as_float(w << 16); }
DI float hi16(unsigned w) { return __uint_as_float(w & 0xffff0000u); }
DI float dot2(unsigned a, unsigned b, float acc) { return __builtin_amdgcn_fdot2_f32_bf16(__builtin_bit_cast(bf16x2, a), __builtin_bit_cast(bf16x2, b), acc, false); }
DI unsigned guard(float f) { unsigned u = __float_as_uint(f); return u < 0xFFFFFFFEu ? u : 0xFFFFFFFEu; }
template <int CTRL_> DI float dpp(float x) { return __builtin_bit_cast(float, __builtin_amdgcn_mov_dpp(__builtin_bit_cast(int, x), CTRL_, 0xf, 0xf, true)); }
template <int CTRL_> DI unsigned dppu(unsigned x) { return (unsigned)__builtin_amdgcn_mov_dpp((int)x, CTRL_, 0xf, 0xf, true); }
constexpr int ROR4 = 0x124, ROR8 = 0x128, ROR1 = 0x121, ROR2 = 0x122;
DI float xrow16_max(float x) {
  auto s = __builtin_amdgcn_permlane16_swap(__float_as_uint(x), __float_as_uint(x), false, false);
  x = fmaxf(__uint_as_float(s[0]), __uint_as_float(s[1]));
  auto t = __builtin_amdgcn_permlane32_swap(__float_as_uint(x), __float_as_uint(x), false, false);
  return fmaxf(__uint_as_float(t[0]), __uint_as_float(t[1]));
}
DI float xhalf_sum(float x) {
  auto t = __builtin_amdgcn_permlane32_swap(__float_as_uint(x), __float_as_uint(x), false, false);
  return __uint_as_float(t[0]) + __uint_as_float(t[1]);
}
DI float xrow16_sum(float x) {
  auto s = __builtin_amdgcn_permlane16_swap(__float_as_uint(x), __float_as_uint(x), false, false);
  x = __uint_as_float(s[0]) + __uint_as_float(s[1]);
  return xhalf_sum(x);
}
DI float wave_sum(float x) { x += dpp<ROR1>(x); x += dpp<ROR2>(x); x += dpp<ROR4>(x); x += dpp<ROR8>(x); return xrow16_sum(x); }
DI float wave_max(float x) { x = fmaxf(x, dpp<ROR1>(x)); x = fmaxf(x, dpp<ROR2>(x)); x = fmaxf(x, dpp<ROR4>(x)); x = fmaxf(x, dpp<ROR8>(x)); return xrow16_max(x); }

DI u64 next_tag(u64 e) { const u64 t = e + 1ull; return (unsigned)t == 0u ? t + 1ull : t; }

#define LDW(p) __builtin_nontemporal_load(reinterpret_cast<const u32x4*>(p))
#define WAITV_(n) asm volatile("s_waitcnt vmcnt(" #n ")" ::: "memory")
#define WAITV(n) WAITV_(n)
#define WAITL0() asm volatile("s_waitcnt lgkmcnt(0)" ::: "memory")
#define BAR() do { asm volatile("s_waitcnt lgkmcnt(0)" ::: "memory"); __builtin_amdgcn_s_barrier(); asm volatile("" ::: "memory"); } while (0)
DI void st_sc1_x4(void* p, u32x4 v) { asm volatile("global_store_dwordx4 %0, %1, off sc1\n\ts_nop 1" :: "v"(p), "v"(v) : "memory"); }
DI void st_sc1_x1(void* p, unsigned v) { asm volatile("global_store_dword %0, %1, off sc1\n\ts_nop 1" :: "v"(p), "v"(v) : "memory"); }
DI unsigned now32() { return (unsigned)__builtin_amdgcn_s_memrealtime(); }
DI void st_sc1_x2(void* p, u32x2 v) { asm volatile("global_store_dwordx2 %0, %1, off sc1\n\ts_nop 1" :: "v"(p), "v"(v) : "memory"); }
DI unsigned rfl(unsigned v) { return (unsigned)__builtin_amdgcn_readfirstlane((int)v); }

#define STAMP(i) do { } while (0)
#define STAMP0(lo, hi) do { } while (0)
#define DUMP(lo, hi) do { } while (0)

#define DMA1(m0a, rs, so, v0)                                                              \
  asm volatile("s_mov_b32 m0, %0\n\ts_nop 0\n\t"                                           \
               "buffer_load_dwordx4 %3, %1, %2 offen nt lds"                               \
               :: "s"(m0a), "s"(rs), "s"(so), "v"(v0) : "memory")
#define DMA4(m0a, rs, so, v0, v1, v2, v3)                                                  \
  asm volatile("s_mov_b32 m0, %0\n\ts_nop 0\n\t"                                           \
               "buffer_load_dwordx4 %3, %1, %2 offen nt lds\n\t"                           \
               "buffer_load_dwordx4 %4, %1, %2 offen offset:1024 nt lds\n\t"               \
               "buffer_load_dwordx4 %5, %1, %2 offen offset:2048 nt lds\n\t"               \
               "buffer_load_dwordx4 %6, %1, %2 offen offset:3072 nt lds"                   \
               :: "s"(m0a), "s"(rs), "s"(so), "v"(v0), "v"(v1), "v"(v2), "v"(v3) : "memory")

#define DMA1K(m0a, rs, so, v0, off)                                                        \
  asm volatile("s_waitcnt vmcnt(%4)\n\ts_mov_b32 m0, %0\n\ts_nop 0\n\t"                    \
               "buffer_load_dwordx4 %3, %1, %2 offen offset:%5 nt lds"                     \
               :: "s"(m0a), "s"(rs), "s"(so), "v"(v0), "n"(NK - 1), "n"(off) : "memory")

#define PUB_(l) "s_mov_b64 exec, 1\n\tv_mov_b32 %[tmp], " l "\n\tds_write_b32 %[fa], %[tmp]\n\ts_mov_b64 exec, -1\n\t"
#define REQ_(v, off) "s_waitcnt vmcnt(%[wm])\n\tbuffer_load_dwordx4 " v ", %[rs], %[so] offen offset:" off " nt lds\n\t"
#define REQ2_(v, off) "s_waitcnt vmcnt(%[wm])\n\tbuffer_load_dwordx4 " v ", %[rs], %[so1] offen offset:" off " nt lds\n\t"
template <int W> DI void dma_tile(unsigned lo, __amdgpu_buffer_rsrc_t rs, unsigned so, const unsigned (&v)[8], unsigned fa, int l0, int l1) {
  unsigned tmp;
  const unsigned so1 = so + 4096u, lo1 = lo + 4096u;
  constexpr int PP = (W - 1) & 3;
#define TILE_ASM(P0, P1, P2, P3, Q0, Q1, Q2, Q3)                                                                  \
  asm volatile("s_mov_b32 m0, %[lo]\n\ts_nop 0\n\t"                                                             \
               REQ_("%[v0]", "0") P0 REQ_("%[v1]", "1024") P1 REQ_("%[v2]", "2048") P2 REQ_("%[v3]", "3072") P3     \
               "s_mov_b32 m0, %[lo1]\n\ts_nop 0\n\t"                                                            \
               REQ2_("%[v4]", "0") Q0 REQ2_("%[v5]", "1024") Q1 REQ2_("%[v6]", "2048") Q2 REQ2_("%[v7]", "3072") Q3 \
               : [tmp] "=&v"(tmp)                                                                                  \
               : [lo] "s"(lo), [lo1] "s"(lo1), [rs] "s"(rs), [so] "s"(so), [so1] "s"(so1),                          \
                 [v0] "v"(v[0]), [v1] "v"(v[1]), [v2] "v"(v[2]), [v3] "v"(v[3]),                                    \
                 [v4] "v"(v[4]), [v5] "v"(v[5]), [v6] "v"(v[6]), [v7] "v"(v[7]),                                    \
                 [fa] "v"(fa), [l0] "s"(l0), [l1] "s"(l1), [wm] "n"(W - 1) : "memory")
  if constexpr (PP == 0) TILE_ASM(PUB_("%[l0]"), "", "", "", PUB_("%[l1]"), "", "", "");
  else if constexpr (PP == 1) TILE_ASM("", PUB_("%[l0]"), "", "", "", PUB_("%[l1]"), "", "");
  else if constexpr (PP == 2) TILE_ASM("", "", PUB_("%[l0]"), "", "", "", PUB_("%[l1]"), "");
  else TILE_ASM("", "", "", PUB_("%[l0]"), "", "", "", PUB_("%[l1]"));
#undef TILE_ASM
}

template <int W> DI void tile_w(unsigned lo, __amdgpu_buffer_rsrc_t rs, unsigned so, const unsigned (&v)[8], unsigned fa, int g, int& L) {
  const int l0 = max(L, g - (W - 1) / 4), l1 = max(L, g + 1 - (W - 1) / 4);
  dma_tile<W>(lo, rs, so, v, fa, l0, l1);
  L = l1;
}
struct SW {
  unsigned ka[4], va[8];
  bf16x8 qf[8];
  f32x4 acc[16];
  float m, l;
};

DI f32x4 qk_part(const SW& c, unsigned koff) {
  bf16x8 kf[8];
#pragma unroll
  for (int s = 0; s < 8; ++s) kf[s] = *(LAS bf16x8*)(size_t)(c.ka[s & 3] + koff + (s >> 2) * 256u);
  f32x4 s0 = {0.f, 0.f, 0.f, 0.f}, s1 = {0.f, 0.f, 0.f, 0.f};
#pragma unroll
  for (int s = 0; s < 8; s += 2) {
    s0 = __builtin_amdgcn_mfma_f32_16x16x32_bf16(kf[s], c.qf[s], s0, 0, 0, 0);
    s1 = __builtin_amdgcn_mfma_f32_16x16x32_bf16(kf[s + 1], c.qf[s + 1], s1, 0, 0, 0);
  }
  return s0 + s1;
}
DI s16x4 soft_part(SW& c, const f32x4 S) {
  float mx = fmaxf(fmaxf(S[0], S[1]), fmaxf(S[2], S[3]));
  mx = xrow16_max(mx);
  if (__any(mx > c.m)) {
    const float mn = fmaxf(c.m, mx);
    const float alpha = __builtin_amdgcn_exp2f((c.m - mn) * C2);
    c.m = mn; c.l *= alpha;
#pragma unroll
    for (int cb = 0; cb < 16; ++cb) c.acc[cb] *= alpha;
  }
  const float mc = c.m * C2;
  const float p0 = __builtin_amdgcn_exp2f(fmaf(S[0], C2, -mc)), p1 = __builtin_amdgcn_exp2f(fmaf(S[1], C2, -mc));
  const float p2 = __builtin_amdgcn_exp2f(fmaf(S[2], C2, -mc)), p3 = __builtin_amdgcn_exp2f(fmaf(S[3], C2, -mc));
  c.l += (p0 + p1) + (p2 + p3);
  u32x2 pk = {cvtpk(p0, p1), cvtpk(p2, p3)};
  return __builtin_bit_cast(s16x4, pk);
}
DI void pv_part(SW& c, const s16x4 P, unsigned voff) {
  s16x4 vf[16];
#pragma unroll
  for (int cb = 0; cb < 16; ++cb) vf[cb] = __builtin_amdgcn_ds_read_tr16_b64_v4i16((LAS s16x4*)(size_t)(c.va[cb & 7] + voff + (cb >> 3) * 256u));
#pragma unroll
  for (int cb = 0; cb < 16; ++cb) c.acc[cb] = __builtin_amdgcn_mfma_f32_16x16x16bf16_1k(vf[cb], P, c.acc[cb], 0, 0, 0);
}

DI void wait_flag(volatile LAS unsigned* f, unsigned need) {
  unsigned spins = 0;
  while (*f < need) { __builtin_amdgcn_s_sleep(1); if (++spins > SPIN_L) break; }
  asm volatile("" ::: "memory");
}
template <int SLOT> DI void tile(SW& c, unsigned t, volatile LAS unsigned* flag, int wave) {
  wait_flag(flag + 14, 16u * t + 2u * wave + 2u);
  const f32x4 S = qk_part(c, SLOT * 8192u);
  const s16x4 P = soft_part(c, S);
  WAITL0();
  if ((threadIdx.x & 63) == 0) flag[4 + wave] = t + 1u;
  wait_flag(flag + 14, 16u * t + 2u * wave + 10u);
  pv_part(c, P, 16384u + SLOT * 8192u);
  WAITL0();
  if ((threadIdx.x & 63) == 0) flag[8 + wave] = t + 1u;
}

static_assert(W0 >= 17 && W0 <= 64 && NK >= 17 && NK <= 64, "window");
struct Args {
  const u16* x; const u16* kc; const u16* vc; const u16* wq; const u16* wo;
  const u16* wg; const u16* wu; const u16* wd; const u16* gw;
  u16* out; unsigned char* ws;
};

DI const CAS Args* kargs() {
  const CAS Args* p = (const CAS Args*)__builtin_amdgcn_kernarg_segment_ptr();
  asm volatile("" : "+s"(p));
  return p;
}

template <int NR> DI void colacc(const u32x4 (&w)[NR], const float (&xv)[NR], float* red, int lane) {
  float acc[8];
#pragma unroll
  for (int j = 0; j < 8; ++j) acc[j] = 0.f;
#pragma unroll
  for (int i = 0; i < NR; ++i)
#pragma unroll
    for (int j = 0; j < 4; ++j) { acc[2 * j] = fmaf(xv[i], lo16(w[i][j]), acc[2 * j]); acc[2 * j + 1] = fmaf(xv[i], hi16(w[i][j]), acc[2 * j + 1]); }
  *reinterpret_cast<f32x4*>(red + lane * 8) = f32x4{acc[0], acc[1], acc[2], acc[3]};
  *reinterpret_cast<f32x4*>(red + lane * 8 + 4) = f32x4{acc[4], acc[5], acc[6], acc[7]};
}
DI float red8(const float* red, int base, int stride) {
  float t = 0.f;
#pragma unroll
  for (int i = 0; i < 8; ++i) t += red[base + i * stride];
  return t;
}

__global__ void __launch_bounds__(384, 1) fused_kernel(Args a) {
  extern __shared__ __attribute__((aligned(16))) unsigned char lds[];
  const int b = blockIdx.x;
  const int h = (b >> 3) & 7, s = (b & 7) + 8 * (b >> 6);
  const int xh = b & 7, xs = b >> 3;
  const int me = h * 32 + s;
  const int tid = threadIdx.x, lane = tid & 63, wave = __builtin_amdgcn_readfirstlane(tid >> 6);
  volatile LAS unsigned* flag = (volatile LAS unsigned*)(size_t)((unsigned)(size_t)lds + L_FLAG);
  unsigned char* in2 = a.ws + WS_IN2;
  unsigned char* yb_ = a.ws + WS_Y;

  u64 E = *(const CAS u64*)(a.ws + WS_E);
  const unsigned ldsb = (unsigned)(size_t)lds;

  if (wave == NW) {
    __builtin_amdgcn_s_setprio(3);
    const unsigned tstart = now32();
    STAMP0(0, 9);
    STAMP(0);
    const int lc = lane < 56 ? lane : 55;
    const unsigned Ls = (3u - (unsigned)((size_t)a.wq >> 7)) & 7u;
    const bool wide = (unsigned)(xs >> 2) == Ls;
    const int r0 = wide ? 112 * (xs & 3) + 14 * (lane >> 3) : 14 * (lane >> 1);
    u32x4 wqr[14], xx, gg, xa, xb;
    {
      const int col = wide ? (xs >> 2) * 64 + (lane & 7) * 8 : xs * 16 + (lane & 1) * 8;
      const u16* wp = a.wq + (size_t)r0 * 4096 + (xh * 512 + col);
#pragma unroll
      for (int i = 0; i < 14; ++i) wqr[i] = LDW(wp + (size_t)i * 4096);
      xa = *reinterpret_cast<const u32x4_a4*>(a.x + r0);
      xb = *reinterpret_cast<const u32x4_a4*>(a.x + r0 + 6);
      xx = *reinterpret_cast<const u32x4*>(a.x + lc * 8); gg = *reinterpret_cast<const u32x4*>(a.gw + lc * 8);
    }
    if (lane < 32) flag[lane] = lane == 15 ? 0x7fffffffu : 0u;
    BAR();
    float xv[14];
#pragma unroll
    for (int i = 0; i < 14; ++i) asm volatile("" : "+v"(wqr[i]));
    asm volatile("" : "+v"(xa), "+v"(xb));
#pragma unroll
    for (int i = 0; i < 4; ++i) { xv[2 * i] = lo16(xa[i]); xv[2 * i + 1] = hi16(xa[i]); }
#pragma unroll
    for (int i = 0; i < 3; ++i) { xv[8 + 2 * i] = lo16(xb[1 + i]); xv[9 + 2 * i] = hi16(xb[1 + i]); }
    float* red = reinterpret_cast<float*>(lds + L_RED);
    asm volatile("" : "+s"(E));
    const u64 T = next_tag(E);
    const u32x2 Tw = {(unsigned)T, (unsigned)(T >> 32)};
    {
      colacc<14>(wqr, xv, red, lane);
      WAITL0();
      float qv = red8(red, wide ? lane : (lane >> 4) * 128 + (lane & 15), wide ? 64 : 16);
      if (wide) {
        st_sc1_x2(a.ws + WS_QP + (size_t)((xh * 4 + (xs & 3)) * 64 + lane) * 8, u32x2{__float_as_uint(qv), Tw[0]});
      } else {
        qv = xrow16_sum(qv);
        const u64 gr = (T << 16) | (u64)(cvtpk(qv, 0.f) & 0xffffu);
        if (lane < 16) st_sc1_x2(a.ws + WS_QG + (size_t)(xh * 512 + xs * 16 + lane) * 8, u32x2{(unsigned)gr, (unsigned)(gr >> 32)});
      }
    }
    if (lane < 56) *reinterpret_cast<u32x4*>(lds + L_X + lane * 16) = xx;
    STAMP(1);
    bool dead = false;
    {
      const unsigned char* qg = a.ws + WS_QG + (size_t)h * 4096 + lane * 16;
      const unsigned char* qp = a.ws + WS_QP + (size_t)h * 2048 + lane * 16;
      const unsigned tlo = (unsigned)(T << 16), thi = (unsigned)(T >> 16);
      const unsigned mine = (unsigned)(lane >> 5);
      auto okg = [&](const u32x4& g, unsigned j) { return (((g[0] & 0xffff0000u) == tlo) & (g[1] == thi) & ((g[2] & 0xffff0000u) == tlo) & (g[3] == thi)) | (2 * j + mine == Ls); };
      auto okp = [&](const u32x4& g) { return ((g[1] == Tw[0]) & (g[3] == Tw[0])) | (Ls > 7u); };
      u32x4 g0, g1, g2, g3, p0, p1, e0, e1, e2, e3, f0, f1;
      while (now32() - tstart < (unsigned)QW0) __builtin_amdgcn_s_sleep(2);
#define QLOADS(a0, a1, a2, a3, b0, b1, tail)                                  \
      asm volatile("global_load_dwordx4 %0, %6, off sc1\n\t"                  \
                   "global_load_dwordx4 %1, %6, off offset:1024 sc1\n\t"      \
                   "global_load_dwordx4 %2, %6, off offset:2048 sc1\n\t"      \
                   "global_load_dwordx4 %3, %6, off offset:3072 sc1\n\t"      \
                   "global_load_dwordx4 %4, %7, off sc1\n\t"                  \
                   "global_load_dwordx4 %5, %7, off offset:1024 sc1" tail     \
                   : "=&v"(a0), "=&v"(a1), "=&v"(a2), "=&v"(a3), "=&v"(b0), "=&v"(b1) : "v"(qg), "v"(qp) : "memory")
      QLOADS(g0, g1, g2, g3, p0, p1, "");
      while (now32() - tstart < (unsigned)(QW0 + QD)) __builtin_amdgcn_s_sleep(1);
      QLOADS(e0, e1, e2, e3, f0, f1, "\n\ts_waitcnt vmcnt(6)");
      asm volatile("" : "+v"(g0), "+v"(g1), "+v"(g2), "+v"(g3), "+v"(p0), "+v"(p1));
      if (__any(!(okg(g0, 0) & okg(g1, 1) & okg(g2, 2) & okg(g3, 3) & okp(p0) & okp(p1)))) {
        WAITV(0);
        asm volatile("" : "+v"(e0), "+v"(e1), "+v"(e2), "+v"(e3), "+v"(f0), "+v"(f1));
        if (!okg(g0, 0)) g0 = e0;
        if (!okg(g1, 1)) g1 = e1;
        if (!okg(g2, 2)) g2 = e2;
        if (!okg(g3, 3)) g3 = e3;
        if (!okp(p0)) p0 = f0;
        if (!okp(p1)) p1 = f1;
        unsigned spins = 0;
        for (;;) {
          const bool need = !(okg(g0, 0) & okg(g1, 1) & okg(g2, 2) & okg(g3, 3) & okp(p0) & okp(p1));
          if (!__any(need)) break;
          if (++spins > SPIN_G) { dead = true; break; }
          if (spins > 1) { const unsigned t1 = now32(); while (now32() - t1 < (unsigned)QW1) __builtin_amdgcn_s_sleep(1); }
          if (need) {
            asm volatile("global_load_dwordx4 %0, %6, off sc1\n\t"
                         "global_load_dwordx4 %1, %6, off offset:1024 sc1\n\t"
                         "global_load_dwordx4 %2, %6, off offset:2048 sc1\n\t"
                         "global_load_dwordx4 %3, %6, off offset:3072 sc1\n\t"
                         "global_load_dwordx4 %4, %7, off sc1\n\t"
                         "global_load_dwordx4 %5, %7, off offset:1024 sc1\n\t"
                         "s_waitcnt vmcnt(0)"
                         : "+&v"(g0), "+&v"(g1), "+&v"(g2), "+&v"(g3), "+&v"(p0), "+&v"(p1) : "v"(qg), "v"(qp) : "memory");
          }
        }
      }
      unsigned* ql = reinterpret_cast<unsigned*>(lds + L_Q);
      ql[lane] = (g0[0] & 0xffffu) | (g0[2] << 16);
      ql[64 + lane] = (g1[0] & 0xffffu) | (g1[2] << 16);
      ql[128 + lane] = (g2[0] & 0xffffu) | (g2[2] << 16);
      ql[192 + lane] = (g3[0] & 0xffffu) | (g3[2] << 16);
      {
        const float qa = xhalf_sum(__uint_as_float(p0[0]) + __uint_as_float(p1[0]));
        const float qb = xhalf_sum(__uint_as_float(p0[2]) + __uint_as_float(p1[2]));
        if (lane < 32) ql[Ls * 32 + lane] = cvtpk(qa, qb);
      }
      WAITL0();
      if (lane == 0) flag[0] = 1u;
      WAITV(0);
      asm volatile("" :: "v"(e0), "v"(e1), "v"(e2), "v"(e3), "v"(f0), "v"(f1));
    }
    STAMP(2);
    const CAS Args* ka = kargs();
    u32x4 wgr[7], wur[7], wdr[8];
    const int lg = b >> 3, ks = b & 7;
    if (b < NFF) {
      const size_t o = (size_t)(56 * ks + (lane >> 3)) * F + lg * 64 + (lane & 7) * 8;
#pragma unroll
      for (int i = 0; i < 7; ++i) { wgr[i] = LDW(ka->wg + o + (size_t)i * 8 * F); wur[i] = LDW(ka->wu + o + (size_t)i * 8 * F); }
#pragma unroll
      for (int u = 0; u < 8; ++u) wdr[u] = LDW(ka->wd + (size_t)(b * 8 + u) * D + lc * 8);
    }
    {
      const u32x4 e4 = {EMPTY, EMPTY, EMPTY, EMPTY};
      unsigned char* p = in2 + (size_t)me * IN2_STRIDE + lane * 16;
      st_sc1_x4(p, e4); st_sc1_x4(p + 1024, e4);
      if (lane < 16) st_sc1_x4(p + 2048, e4);
      unsigned char* q = yb_ + (size_t)b * 2048 + lane * 16;
      st_sc1_x4(q, e4);
      if (lane < 48) st_sc1_x4(q + 1024, e4);
    }
    {
      const int o0 = (2 * h + (s >> 4)) * 256 + (s & 15) * 16;
      const __amdgpu_buffer_rsrc_t rwo = __builtin_amdgcn_make_buffer_rsrc((void*)(ka->wo + (size_t)o0 * D), 0, 16 * D * 2, 0x00020000);
      const unsigned vl16 = lane * 16u;
#pragma unroll
      for (int k = 0; k < 14; ++k) DMA1(ldsb + L_W + k * 1024u, rwo, k * 1024u, vl16);
    }
    WAITV(0);
    if (lane == 0) st_sc1_x2(a.ws + WS_RDY + (size_t)b * 8, Tw);
    __builtin_amdgcn_s_setprio(0);
    {
      float t = 0.f;
#pragma unroll
      for (int j = 0; j < 4; ++j) { t += rbf(lo16(xx[j]) * lo16(gg[j])); t += rbf(hi16(xx[j]) * hi16(gg[j])); }
      if (lane >= 56) t = 0.f;
      t = rbf(wave_sum(t));
      const float g = rbf(1.f / (1.f + __expf(-t)));
      float yb[8];
#pragma unroll
      for (int j = 0; j < 8; ++j) yb[j] = 0.f;
      if (b < NFF) {
        float xr[7];
#pragma unroll
        for (int i = 0; i < 7; ++i) xr[i] = lo16(reinterpret_cast<const u16*>(lds + L_X)[56 * ks + 8 * i + (lane >> 3)]);
        colacc<7>(wgr, xr, red, lane);
        colacc<7>(wur, xr, red + 512, lane);
        WAITL0();
        const float pg = red8(red, lane, 64), pu = red8(red + 512, lane, 64);
        st_sc1_x4(a.ws + WS_FP + (size_t)(b * 64 + lane) * 16, u32x4{__float_as_uint(pg), Tw[0], __float_as_uint(pu), Tw[0]});
        const unsigned char* fp = a.ws + WS_FP + (size_t)((lg * 8 + (lane >> 3)) * 64 + ks * 8 + (lane & 7)) * 16;
        u32x4 f = {0u, ~Tw[0], 0u, ~Tw[0]};
        unsigned spins = 0;
        for (;;) {
          const bool need = (f[1] != Tw[0]) | (f[3] != Tw[0]);
          if (!__any(need)) break;
          if (dead || ++spins > SPIN_G) { dead = true; break; }
          { const unsigned t1 = now32(); while (now32() - t1 < 60u) __builtin_amdgcn_s_sleep(2); }
          if (need) asm volatile("global_load_dwordx4 %0, %1, off sc1\n\ts_waitcnt vmcnt(0)" : "+&v"(f) : "v"(fp) : "memory");
        }
        float gs = __uint_as_float(f[0]), us = __uint_as_float(f[2]);
        gs += dpp<ROR8>(gs); us += dpp<ROR8>(us);
        const float r = rbf(xrow16_sum(gs)), up = rbf(xrow16_sum(us));
        const float tt = tanhf(0.7978845608028654f * (r + 0.044715f * r * r * r));
        const float hv = rbf(rbf(0.5f * r * (1.f + tt)) * up);
#pragma unroll
        for (int u = 0; u < 8; ++u) {
          const float hu = __builtin_bit_cast(float, __builtin_amdgcn_readlane(__builtin_bit_cast(int, hv), u));
#pragma unroll
          for (int j = 0; j < 4; ++j) { yb[2 * j] = fmaf(hu, lo16(wdr[u][j]), yb[2 * j]); yb[2 * j + 1] = fmaf(hu, hi16(wdr[u][j]), yb[2 * j + 1]); }
        }
      }
      const float omg = rbf(1.f - g);
      float* yl = reinterpret_cast<float*>(lds + L_YB);
      if (lane < 56) {
        *reinterpret_cast<f32x4*>(yl + lane * 8) = f32x4{yb[0] * omg, yb[1] * omg, yb[2] * omg, yb[3] * omg};
        *reinterpret_cast<f32x4*>(yl + lane * 8 + 4) = f32x4{yb[4] * omg, yb[5] * omg, yb[6] * omg, yb[7] * omg};
      }
      if (lane == 0) yl[448] = g;
      WAITL0();
      if (lane == 0) flag[12] = 1u;
    }
    STAMP(3);
    if (b == MASTER) {
      const unsigned char* rp = a.ws + WS_RDY + lane * 16;
      unsigned spins = 0;
      for (;;) {
        u32x4 q0, q1;
        asm volatile("global_load_dwordx4 %0, %2, off sc1\n\tglobal_load_dwordx4 %1, %2, off offset:1024 sc1\n\ts_waitcnt vmcnt(0)"
                     : "=&v"(q0), "=&v"(q1) : "v"(rp) : "memory");
        const unsigned tl = Tw[0], th = Tw[1];
        const bool ok = (q0[0] == tl) & (q0[1] == th) & (q0[2] == tl) & (q0[3] == th) & (q1[0] == tl) & (q1[1] == th) & (q1[2] == tl) & (q1[3] == th);
        if (__all(ok)) break;
        if (dead || ++spins > SPIN_G) { dead = true; break; }
        __builtin_amdgcn_s_sleep(8);
      }
      if (lane < 32) st_sc1_x2(a.ws + WS_BC + lane * 512, Tw);
    }
    {
      const unsigned char* pp = a.ws + WS_BC + (b & 31) * 512;
      unsigned spins = 0;
      for (;;) {
        u32x2 ent;
        asm volatile("global_load_dwordx2 %0, %1, off sc1\n\ts_waitcnt vmcnt(0)" : "=&v"(ent) : "v"(pp) : "memory");
        if (rfl(ent[0]) == Tw[0] && rfl(ent[1]) == Tw[1]) break;
        if (dead || ++spins > SPIN_G) break;
        { const unsigned t1 = now32(); while (now32() - t1 < 200u) __builtin_amdgcn_s_sleep(8); }
      }
      if (lane == 0) flag[13] = 1u;
    }
    {
      const bool rep = NREP == 32 ? ((b >> 3) & 7) == 0 : NREP == 16 ? ((b >> 3) & 15) == 5 : (b >> 3) == 13;
      const int ridx = NREP == 32 ? (b & 7) + 8 * (b >> 6) : NREP == 16 ? (b & 7) + 8 * (b >> 7) : (b & 7);
      constexpr int PC = 16 * CKR;
      int c = CK0, rd = -1;
      unsigned tc = 0, T0 = 0, Tf = 0;
      int cf = 0;
      bool have = false;
      const float phi = 2.f * (float)((b * 157) & 255) * (1.f / 256.f) - 1.f;
      float A0 = 0.f, rho = RHO0;
      for (unsigned spins = 0; spins < 4000u; ++spins) {
        const unsigned L = rfl(flag[14]);
        const unsigned t = now32();
        if (L >= (unsigned)(16 * NT - 12)) break;
        if (c < NT / CKR && L >= (unsigned)(PC * c)) {
          tc = t; rd = c; ++c;
          if (rep && lane < RSTR / 16) st_sc1_x4(a.ws + WS_PR + (size_t)rd * (32 * RSTR) + ridx * RSTR + lane * 16, u32x4{Tw[0], tc, Tw[0], tc});
        }
        if (rd >= 0 && (int)(t - tc) >= DLY) {
          if (have && lane == 0) flag[15] = (unsigned)(A0 + (float)(int)(t + 150u - T0) * rho);
          u32x2 ent;
          asm volatile("global_load_dwordx2 %0, %1, off sc1\n\ts_waitcnt vmcnt(0)" : "=&v"(ent)
                       : "v"(a.ws + WS_PR + (size_t)rd * (32 * RSTR) + (lane & (NREP - 1)) * RSTR) : "memory");
          const int dn = min((int)(now32() - tc), 2 * DLY);
          int d = (int)(ent[1] - tc);
          d = ent[0] == Tw[0] ? max(min(d, 1000), -1000) : dn;
          const int lead = (int)rfl((unsigned)(int)(wave_sum((float)d) * (1.f / 64.f)));
          const unsigned Tn = tc + (unsigned)lead;
          if (!have) { Tf = Tn; cf = rd; }
          else if (!FIXRHO) {
            const float per = fminf(fmaxf((float)(int)(Tn - Tf) / (float)(rd - cf), (float)PC * 13.f), (float)PC * 17.f);
            rho = (float)PC * ACC / per;
          }
          T0 = Tn; A0 = (float)(PC * rd + OFFP) + phi; have = true; rd = -1;
        } else {
          if (have && lane == 0) flag[15] = (unsigned)(A0 + (float)(int)(t - T0) * rho);
        }
        __builtin_amdgcn_s_sleep(1);
      }
      if (lane == 0) flag[15] = 0x7fffffffu;
    }
    __builtin_amdgcn_s_setprio(3);
    STAMP(4);
    if (b < NC3) {
      { unsigned spins = 0; while (flag[3] < 4u) { __builtin_amdgcn_s_sleep(2); if (++spins > SPIN_L) break; } }
      STAMP(6);
      const unsigned char* p = yb_ + (size_t)lane * 2048 + b * 16;
      constexpr int RS = 64 * 2048;
      const unsigned char* p1 = p + RS; const unsigned char* p2 = p + 2 * RS; const unsigned char* p3 = p + 3 * RS;
      u32x4 y0 = {EMPTY, EMPTY, EMPTY, EMPTY}, y1 = y0, y2 = y0, y3 = y0;
      unsigned spins = 0;
      for (;;) {
        auto bad = [&](const u32x4& y) { return (y[0] == EMPTY) | (y[1] == EMPTY) | (y[2] == EMPTY) | (y[3] == EMPTY); };
        const bool need = bad(y0) | bad(y1) | bad(y2) | bad(y3);
        if (!__any(need)) break;
        if (dead || ++spins > SPIN_G) break;
        if (need) {
          asm volatile("global_load_dwordx4 %0, %4, off sc1\n\t"
                       "global_load_dwordx4 %1, %5, off sc1\n\t"
                       "global_load_dwordx4 %2, %6, off sc1\n\t"
                       "global_load_dwordx4 %3, %7, off sc1\n\t"
                       "s_waitcnt vmcnt(0)"
                       : "+&v"(y0), "+&v"(y1), "+&v"(y2), "+&v"(y3) : "v"(p), "v"(p1), "v"(p2), "v"(p3) : "memory");
        }
      }
      STAMP(7);
      float v[4];
#pragma unroll
      for (int j = 0; j < 4; ++j) v[j] = wave_sum((__uint_as_float(y0[j]) + __uint_as_float(y1[j])) + (__uint_as_float(y2[j]) + __uint_as_float(y3[j])));
      if (lane == 0) {
        *reinterpret_cast<u32x2*>(a.out + b * 4) = u32x2{cvtpk(v[0], v[1]), cvtpk(v[2], v[3])};
        if (b == 0) st_sc1_x2(a.ws + WS_E, Tw);
      }
      STAMP(8);
    }
    DUMP(0, 9);
    return;
  }

  const unsigned hi = lane >> 5, pc = lane & 31;
  if (wave == NW + 1) {
    __builtin_amdgcn_s_setprio(2);
    const size_t base = ((size_t)h * SK + (size_t)s * TOK_PER_WG) * DH * 2;
    const __amdgpu_buffer_rsrc_t rk = __builtin_amdgcn_make_buffer_rsrc((void*)((const char*)a.kc + base), 0, TOK_PER_WG * DH * 2, 0x00020000);
    const __amdgpu_buffer_rsrc_t rv = __builtin_amdgcn_make_buffer_rsrc((void*)((const char*)a.vc + base), 0, TOK_PER_WG * DH * 2, 0x00020000);
    unsigned vk[8], vv[8];
#pragma unroll
    for (int j = 0; j < 8; ++j) vk[j] = (hi * 512u + ((pc ^ hi) << 4)) ^ (unsigned)(j << 5);
#pragma unroll
    for (int j = 0; j < 8; ++j) vv[j] = (hi * 512u + ((pc ^ (hi << 1)) << 4)) ^ (unsigned)((j & 3) << 6);
    unsigned vt0;
    asm volatile("buffer_load_dword %0, %1, %2, 0 offen" : "=&v"(vt0) : "v"(0u), "s"(rv) : "memory");
    const unsigned fa = ldsb + L_FLAG + 14 * 4;
    int L = 0;
#pragma unroll 1
    for (int t = 0; t < 2; ++t) {
      const unsigned lo0 = ldsb + L_RING + t * 8192u;
#pragma unroll 1
      for (int w = 0; w < NW; ++w) {
        if (t == 0 && w == 2) BAR();
        const int g = 16 * t + 2 * w;
        const int l0 = max(L, g - (W0 - 1) / 4), l1 = max(L, g + 1 - (W0 - 1) / 4);
        dma_tile<W0>(lo0 + w * 32768u, rk, (unsigned)(t * NW + w) * 8192u, vk, fa, l0, l1);
        L = l1;
      }
#pragma unroll 1
      for (int w = 0; w < NW; ++w) {
        const int g = 16 * t + 8 + 2 * w;
        const int l0 = max(L, g - (W0 - 1) / 4), l1 = max(L, g + 1 - (W0 - 1) / 4);
        dma_tile<W0>(lo0 + w * 32768u + 16384u, rv, (unsigned)(t * NW + w) * 8192u, vv, fa, l0, l1);
        L = l1;
      }
    }
    unsigned fv = flag[4];
    unsigned av = flag[15];
    auto pace = [&](int g) {
      if ((unsigned)g > rfl(av)) {
        unsigned sp = 0;
        while ((unsigned)g > rfl(flag[15])) { __builtin_amdgcn_s_sleep(1); if (++sp > 2000u) break; }
      }
      av = flag[15];
    };
    auto slot = [&](int j, int t, int g) {
      if (rfl(fv) < (unsigned)(t - 1)) {
        if (rfl(flag[4 + j]) < (unsigned)(t - 1)) {
          WAITV(0);
          L = g;
          if (lane == 0) flag[14] = (unsigned)L;
          wait_flag(flag + 4 + j, (unsigned)(t - 1));
        }
      }
      fv = flag[4 + ((j + 1) & 7)];
    };
#pragma unroll 1
    for (int t = 2; t < NT; ++t) {
      const unsigned lo0 = ldsb + L_RING + (t & 1) * 8192u;
#pragma unroll 1
      for (int w = 0; w < NW; ++w) {
        const int g = 16 * t + 2 * w;
        slot(w, t, g);
        pace(g);
        tile_w<NK>(lo0 + w * 32768u, rk, (unsigned)(t * NW + w) * 8192u, vk, fa, g, L);
      }
#pragma unroll 1
      for (int w = 0; w < NW; ++w) {
        const int g = 16 * t + 8 + 2 * w;
        slot(4 + w, t, g);
        pace(g);
        tile_w<NK>(lo0 + w * 32768u + 16384u, rv, (unsigned)(t * NW + w) * 8192u, vv, fa, g, L);
      }
    }
    asm volatile("" :: "v"(vt0));
#pragma unroll
    for (int k = (NK + 2) / 4; k >= 0; --k) {
      asm volatile("s_waitcnt vmcnt(%0)" :: "n"(4 * k) : "memory");
      if (16 * NT - k > L) { L = 16 * NT - k; if (lane == 0) flag[14] = (unsigned)L; }
    }
    return;
  }

  SW c;
  const unsigned m0base = ldsb + L_RING + wave * 32768u;
  const unsigned cc = lane & 15, g = lane >> 4;
#pragma unroll
  for (int q = 0; q < 4; ++q) c.ka[q] = m0base + cc * 512u + ((g ^ (cc & 3)) << 4) + ((q ^ (cc >> 2)) << 6);
  {
    const unsigned qq = cc >> 2, p = cc & 3, t = 4 * g + qq;
#pragma unroll
    for (int e = 0; e < 8; ++e) c.va[e] = m0base + t * 512u + ((e ^ (t & 7)) << 5) + p * 8u;
  }
  const int SB = 16 + 12 * wave;
  STAMP0(SB, SB + 12);
  STAMP0(9 + wave, 10 + wave);
  BAR();
  wait_flag(flag, 1u);
  STAMP(SB + 1);
#pragma unroll
  for (int st = 0; st < 8; ++st) {
    const u32x4 qr = *reinterpret_cast<const u32x4*>(lds + L_Q + (cc & 1) * 512 + 64 * st + 16 * g);
    const u32x4 z = {0u, 0u, 0u, 0u};
    c.qf[st] = __builtin_bit_cast(bf16x8, cc < 2 ? qr : z);
  }
  c.m = -INFINITY; c.l = 0.f;
#pragma unroll
  for (int cb = 0; cb < 16; ++cb) c.acc[cb] = f32x4{0.f, 0.f, 0.f, 0.f};
  for (unsigned t = 0; t < (unsigned)NT; t += 2) {
    tile<0>(c, t, flag, wave);
    tile<1>(c, t + 1, flag, wave);
  }
  STAMP(SB + 9);
  const float lsum = xrow16_sum(c.l);
  {
    float* mrg = reinterpret_cast<float*>(lds + L_RING + wave * 32768u);
    if (cc < 2) {
#pragma unroll
      for (int cb = 0; cb < 16; ++cb) *reinterpret_cast<f32x4*>(mrg + cc * 260 + 4 + 16 * cb + 4 * g) = c.acc[cb];
      if (g == 0) { mrg[cc * 260] = c.m; mrg[cc * 260 + 1] = lsum; }
    }
  }
  WAITL0();
  if (lane == 0) __hip_atomic_fetch_add((LAS unsigned*)(flag + 2), 1u, __ATOMIC_RELAXED, __HIP_MEMORY_SCOPE_WORKGROUP);
  { unsigned spins = 0; while (flag[2] < 4u || flag[13] == 0u || flag[12] == 0u) { if (++spins > SPIN_L) break; } }
  asm volatile("" ::: "memory");
  if (wave < 3) {
    const int r = wave < 2 ? wave : (lane >> 4) & 1;
    float mw[NW], M = -INFINITY;
#pragma unroll
    for (int w = 0; w < NW; ++w) { mw[w] = reinterpret_cast<const float*>(lds + L_RING + w * 32768u)[r * 260]; M = fmaxf(M, mw[w]); }
    float wt[NW];
#pragma unroll
    for (int w = 0; w < NW; ++w) wt[w] = __builtin_amdgcn_exp2f((mw[w] - M) * C2);
    if (wave < 2) {
      const int d4 = lane * 4;
      f32x4 o = {0.f, 0.f, 0.f, 0.f};
#pragma unroll
      for (int w = 0; w < NW; ++w) o += wt[w] * *reinterpret_cast<const f32x4*>(reinterpret_cast<const float*>(lds + L_RING + w * 32768u) + r * 260 + 4 + d4);
      const int cons = h * 32 + r * 16 + (d4 >> 4);
      st_sc1_x4(in2 + (size_t)cons * IN2_STRIDE + s * 64 + (d4 & 15) * 4, u32x4{guard(o[0]), guard(o[1]), guard(o[2]), guard(o[3])});
    } else if (lane < 32) {
      float L = 0.f;
#pragma unroll
      for (int w = 0; w < NW; ++w) L = fmaf(wt[w], reinterpret_cast<const float*>(lds + L_RING + w * 32768u)[r * 260 + 1], L);
      const int cons = h * 32 + lane;
      st_sc1_x2(in2 + (size_t)cons * IN2_STRIDE + 2048 + (s & 15) * 16 + (s >> 4) * 8, u32x2{guard(M), guard(L)});
    }
  }
  STAMP(SB + 10);
  {
    const int lc = lane < 56 ? lane : 55;
    unsigned wp2[8][2];
    {
      const unsigned* wl = reinterpret_cast<const unsigned*>(lds + L_W) + wave * 56 + lc;
#pragma unroll
      for (int p = 0; p < 8; ++p) {
        const unsigned r0 = wl[(2 * p) * 224], r1 = wl[(2 * p + 1) * 224];
        wp2[p][0] = __builtin_amdgcn_perm(r1, r0, 0x05040100u);
        wp2[p][1] = __builtin_amdgcn_perm(r1, r0, 0x07060302u);
      }
    }
    const f32x2 ybv = *reinterpret_cast<const f32x2*>(lds + L_YB + (wave * 112 + lc * 2) * 4);
    const float gate = *reinterpret_cast<const float*>(lds + L_YB + 448 * 4);
    const unsigned char* ip = in2 + (size_t)me * IN2_STRIDE;
    const unsigned char* po = ip + lane * 16;
    const unsigned char* pm = ip + 2048 + (lane >> 2) * 16;
    u32x4 oa = {EMPTY, EMPTY, EMPTY, EMPTY}, ob = oa, ml = oa;
    unsigned spins = 0;
    for (;;) {
      auto bad = [&](const u32x4& y) { return (y[0] == EMPTY) | (y[1] == EMPTY) | (y[2] == EMPTY) | (y[3] == EMPTY); };
      const bool need = bad(oa) | bad(ob) | bad(ml);
      if (!__any(need)) break;
      if (++spins > SPIN_G) break;
      if (need) {
        asm volatile("global_load_dwordx4 %0, %3, off sc1\n\t"
                     "global_load_dwordx4 %1, %3, off offset:1024 sc1\n\t"
                     "global_load_dwordx4 %2, %4, off sc1\n\t"
                     "s_waitcnt vmcnt(0)"
                     : "+&v"(oa), "+&v"(ob), "+&v"(ml) : "v"(po), "v"(pm) : "memory");
      }
    }
    STAMP(9 + wave);
    const float m_a = __uint_as_float(ml[0]), l_a = __uint_as_float(ml[1]), m_b = __uint_as_float(ml[2]), l_b = __uint_as_float(ml[3]);
    const float M = wave_max(fmaxf(m_a, m_b));
    const float wa = __builtin_amdgcn_exp2f((m_a - M) * C2), wb = __builtin_amdgcn_exp2f((m_b - M) * C2);
    const float L = wave_sum(fmaf(wa, l_a, wb * l_b)) * 0.25f;
    float o4[4];
#pragma unroll
    for (int j = 0; j < 4; ++j) {
      float t = fmaf(wa, __uint_as_float(oa[j]), wb * __uint_as_float(ob[j]));
      t += dpp<ROR4>(t); t += dpp<ROR8>(t);
      o4[j] = xrow16_sum(t);
    }
    const float inv = 1.f / L;
    const unsigned p01 = cvtpk(o4[0] * inv, o4[1] * inv), p23 = cvtpk(o4[2] * inv, o4[3] * inv);
    unsigned op[8];
    op[0] = dppu<0x00>(p01); op[1] = dppu<0x00>(p23); op[2] = dppu<0x55>(p01); op[3] = dppu<0x55>(p23);
    op[4] = dppu<0xAA>(p01); op[5] = dppu<0xAA>(p23); op[6] = dppu<0xFF>(p01); op[7] = dppu<0xFF>(p23);
    float y0 = 0.f, y1 = 0.f;
#pragma unroll
    for (int p = 0; p < 8; ++p) { y0 = dot2(wp2[p][0], op[p], y0); y1 = dot2(wp2[p][1], op[p], y1); }
    y0 = fmaf(gate, y0, ybv[0]); y1 = fmaf(gate, y1, ybv[1]);
    if (lane < 56) {
      st_sc1_x2(yb_ + (size_t)b * 2048 + (wave * 112 + lane * 2) * 4, u32x2{guard(y0), guard(y1)});
    }
  }
  STAMP(SB + 11);
  if (b < NC3 && lane == 0) __hip_atomic_fetch_add((LAS unsigned*)(flag + 3), 1u, __ATOMIC_RELAXED, __HIP_MEMORY_SCOPE_WORKGROUP);
  DUMP(SB, SB + 12);
  DUMP(9 + wave, 10 + wave);
}

extern "C" void kernel_launch(void* const* d_in, const int* in_sizes, int n_in, void* d_out, int out_size, void* d_ws, size_t ws_size, hipStream_t stream) {
  Args a;
  a.x = (const u16*)d_in[0]; a.kc = (const u16*)d_in[1]; a.vc = (const u16*)d_in[2]; a.wq = (const u16*)d_in[3]; a.wo = (const u16*)d_in[4];
  a.wg = (const u16*)d_in[6]; a.wu = (const u16*)d_in[7]; a.wd = (const u16*)d_in[8]; a.gw = (const u16*)d_in[9];
  a.out = (u16*)d_out; a.ws = (unsigned char*)d_ws;
  (void)hipFuncSetAttribute((const void*)fused_kernel, hipFuncAttributeMaxDynamicSharedMemorySize, LDS_BYTES);
  fused_kernel<<<NWG, (NW + 2) * 64, LDS_BYTES, stream>>>(a);
}
```

```cpp
#include <hip/hip_runtime.h>
#include <cstdint>

using bf16x2 = __attribute__((ext_vector_type(2))) __bf16;
using bf16x8 = __attribute__((ext_vector_type(8))) __bf16;
using s16x4  = __attribute__((ext_vector_type(4))) short;
using u32x4  = __attribute__((ext_vector_type(4))) unsigned;
using u32x2  = __attribute__((ext_vector_type(2))) unsigned;
using f32x4  = __attribute__((ext_vector_type(4))) float;
using f32x2  = __attribute__((ext_vector_type(2))) float;
typedef u32x4 u32x4_a4 __attribute__((aligned(4)));
typedef unsigned long long u64;
typedef unsigned short u16;
#define LAS __attribute__((address_space(3)))
#define GAS __attribute__((address_space(1)))
#define CAS __attribute__((address_space(4)))
#define DI __device__ __forceinline__

constexpr int D = 448, HQ = 16, HKV = 8, SK = 65536, DH = 256, F = 1792;
constexpr int NSPLIT = 32;
constexpr int TOK_PER_WG = SK / NSPLIT;
constexpr int NW = 4;
constexpr int NT = TOK_PER_WG / 16 / NW;
constexpr int NP = 4 * NT;
constexpr int NWG = HKV * NSPLIT;
constexpr int NC3 = 28;
constexpr int NFF = 224;
constexpr float C2 = 0.0625f * 1.4426950408889634f;

#define NK 21
#define W0 21
#define MARG 3
#define OFFP ((NK - 1) / 4 + 1 + MARG)
#define NKB 32
#define BTH 6
#define FIXRHO 0
#define ACC 1.04f
#define RHO0 (16.f / 210.f)
#define CKR 2
#define CK0 3
#define DLY 250
#define NREP 16
#define RSTR 64
#define EBW 2
#define MASTER 255
#define QW0 270
#define QD 40
#define QW1 30
constexpr size_t WS_E = 0;
constexpr size_t WS_RDY = 4096;
constexpr size_t WS_QG = 8192;
constexpr size_t WS_BC = 40960;
constexpr size_t WS_IN2 = 65536;
constexpr size_t IN2_STRIDE = 4096;
constexpr size_t WS_Y = WS_IN2 + 256 * IN2_STRIDE;
constexpr size_t WS_QP = 0x1C0000;
constexpr size_t WS_PR = 0x200000;
constexpr size_t WS_FP = 0x1C8000;
constexpr unsigned L_RING = 0;
constexpr unsigned L_Q = 131072;
constexpr unsigned L_RED = L_Q + 1024;
constexpr unsigned L_W = L_RED + 4096;
constexpr unsigned L_X = L_W + 14336;
constexpr unsigned L_YB = L_X + 1024;
constexpr unsigned L_FLAG = L_YB + 1856;
constexpr unsigned LDS_BYTES = L_FLAG + 128 + 512;
constexpr unsigned EMPTY = 0xFFFFFFFFu;
constexpr unsigned SPIN_G = 1u << 17, SPIN_L = 1u << 22;

DI unsigned cvtpk(float lo, float hi) { f32x2 v = {lo, hi}; bf16x2 b = __builtin_convertvector(v, bf16x2); return __builtin_bit_cast(unsigned, b); }
DI float rbf(float f) { return __uint_as_float(cvtpk(f, 0.f) << 16); }
DI float lo16(unsigned w) { return __uint_as_float(w << 16); }
DI float hi16(unsigned w) { return __uint_as_float(w & 0xffff0000u); }
DI float dot2(unsigned a, unsigned b, float acc) { return __builtin_amdgcn_fdot2_f32_bf16(__builtin_bit_cast(bf16x2, a), __builtin_bit_cast(bf16x2, b), acc, false); }
DI unsigned guard(float f) { unsigned u = __float_as_uint(f); return u < 0xFFFFFFFEu ? u : 0xFFFFFFFEu; }
template <int CTRL_> DI float dpp(float x) { return __builtin_bit_cast(float, __builtin_amdgcn_mov_dpp(__builtin_bit_cast(int, x), CTRL_, 0xf, 0xf, true)); }
constexpr int ROR4 = 0x124, ROR8 = 0x128, ROR1 = 0x121, ROR2 = 0x122;
DI float xrow16_max(float x) {
  auto s = __builtin_amdgcn_permlane16_swap(__float_as_uint(x), __float_as_uint(x), false, false);
  x = fmaxf(__uint_as_float(s[0]), __uint_as_float(s[1]));
  auto t = __builtin_amdgcn_permlane32_swap(__float_as_uint(x), __float_as_uint(x), false, false);
  return fmaxf(__uint_as_float(t[0]), __uint_as_float(t[1]));
}
DI float xhalf_sum(float x) {
  auto t = __builtin_amdgcn_permlane32_swap(__float_as_uint(x), __float_as_uint(x), false, false);
  return __uint_as_float(t[0]) + __uint_as_float(t[1]);
}
DI float xrow16_sum(float x) {
  auto s = __builtin_amdgcn_permlane16_swap(__float_as_uint(x), __float_as_uint(x), false, false);
  x = __uint_as_float(s[0]) + __uint_as_float(s[1]);
  return xhalf_sum(x);
}
DI float wave_sum(float x) { x += dpp<ROR1>(x); x += dpp<ROR2>(x); x += dpp<ROR4>(x); x += dpp<ROR8>(x); return xrow16_sum(x); }

DI u64 next_tag(u64 e) { const u64 t = e + 1ull; return (unsigned)t == 0u ? t + 1ull : t; }

#define LDW(p) __builtin_nontemporal_load(reinterpret_cast<const u32x4*>(p))
#define WAITV_(n) asm volatile("s_waitcnt vmcnt(" #n ")" ::: "memory")
#define WAITV(n) WAITV_(n)
#define WAITL0() asm volatile("s_waitcnt lgkmcnt(0)" ::: "memory")
#define BAR() do { asm volatile("s_waitcnt lgkmcnt(0)" ::: "memory"); __builtin_amdgcn_s_barrier(); asm volatile("" ::: "memory"); } while (0)
DI void st_sc1_x4(void* p, u32x4 v) { asm volatile("global_store_dwordx4 %0, %1, off sc1\n\ts_nop 1" :: "v"(p), "v"(v) : "memory"); }
DI unsigned now32() { return (unsigned)__builtin_amdgcn_s_memrealtime(); }
DI void st_sc1_x2(void* p, u32x2 v) { asm volatile("global_store_dwordx2 %0, %1, off sc1\n\ts_nop 1" :: "v"(p), "v"(v) : "memory"); }
DI unsigned rfl(unsigned v) { return (unsigned)__builtin_amdgcn_readfirstlane((int)v); }


#define DMA1(m0a, rs, so, v0)                                                              \
  asm volatile("s_mov_b32 m0, %0\n\ts_nop 0\n\t"                                           \
               "buffer_load_dwordx4 %3, %1, %2 offen nt lds"                               \
               :: "s"(m0a), "s"(rs), "s"(so), "v"(v0) : "memory")
#define PUB_(l) "s_mov_b64 exec, 1\n\tv_mov_b32 %[tmp], " l "\n\tds_write_b32 %[fa], %[tmp]\n\ts_mov_b64 exec, -1\n\t"
#define REQ_(v, off) "s_waitcnt vmcnt(%[wm])\n\tbuffer_load_dwordx4 " v ", %[rs], %[so] offen offset:" off " nt lds\n\t"
#define REQ2_(v, off) "s_waitcnt vmcnt(%[wm])\n\tbuffer_load_dwordx4 " v ", %[rs], %[so1] offen offset:" off " nt lds\n\t"
template <int W> DI void dma_tile(unsigned lo, __amdgpu_buffer_rsrc_t rs, unsigned so, const unsigned (&v)[8], unsigned fa, int l0, int l1) {
  unsigned tmp;
  const unsigned so1 = so + 4096u, lo1 = lo + 4096u;
  constexpr int PP = (W - 1) & 3;
#define TILE_ASM(P0, P1, P2, P3, Q0, Q1, Q2, Q3)                                                                  \
  asm volatile("s_mov_b32 m0, %[lo]\n\ts_nop 0\n\t"                                                             \
               REQ_("%[v0]", "0") P0 REQ_("%[v1]", "1024") P1 REQ_("%[v2]", "2048") P2 REQ_("%[v3]", "3072") P3     \
               "s_mov_b32 m0, %[lo1]\n\ts_nop 0\n\t"                                                            \
               REQ2_("%[v4]", "0") Q0 REQ2_("%[v5]", "1024") Q1 REQ2_("%[v6]", "2048") Q2 REQ2_("%[v7]", "3072") Q3 \
               : [tmp] "=&v"(tmp)                                                                                  \
               : [lo] "s"(lo), [lo1] "s"(lo1), [rs] "s"(rs), [so] "s"(so), [so1] "s"(so1),                          \
                 [v0] "v"(v[0]), [v1] "v"(v[1]), [v2] "v"(v[2]), [v3] "v"(v[3]),                                    \
                 [v4] "v"(v[4]), [v5] "v"(v[5]), [v6] "v"(v[6]), [v7] "v"(v[7]),                                    \
                 [fa] "v"(fa), [l0] "s"(l0), [l1] "s"(l1), [wm] "n"(W - 1) : "memory")
  if constexpr (PP == 0) TILE_ASM(PUB_("%[l0]"), "", "", "", PUB_("%[l1]"), "", "", "");
  else if constexpr (PP == 1) TILE_ASM("", PUB_("%[l0]"), "", "", "", PUB_("%[l1]"), "", "");
  else if constexpr (PP == 2) TILE_ASM("", "", PUB_("%[l0]"), "", "", "", PUB_("%[l1]"), "");
  else TILE_ASM("", "", "", PUB_("%[l0]"), "", "", "", PUB_("%[l1]"));
#undef TILE_ASM
}

template <int W> DI void tile_w(unsigned lo, __amdgpu_buffer_rsrc_t rs, unsigned so, const unsigned (&v)[8], unsigned fa, int g, int& L) {
  const int l0 = max(L, g - (W - 1) / 4), l1 = max(L, g + 1 - (W - 1) / 4);
  dma_tile<W>(lo, rs, so, v, fa, l0, l1);
  L = l1;
}
struct SW {
  unsigned ka[4], va[8];
  bf16x8 qf[8];
  f32x4 acc[16];
  float m, l;
};

DI f32x4 qk_part(const SW& c, unsigned koff) {
  bf16x8 kf[8];
#pragma unroll
  for (int s = 0; s < 8; ++s) kf[s] = *(LAS bf16x8*)(size_t)(c.ka[s & 3] + koff + (s >> 2) * 256u);
  __builtin_amdgcn_sched_barrier(0);
  f32x4 s0 = {0.f, 0.f, 0.f, 0.f}, s1 = {0.f, 0.f, 0.f, 0.f};
#pragma unroll
  for (int s = 0; s < 8; s += 2) {
    s0 = __builtin_amdgcn_mfma_f32_16x16x32_bf16(kf[s], c.qf[s], s0, 0, 0, 0);
    s1 = __builtin_amdgcn_mfma_f32_16x16x32_bf16(kf[s + 1], c.qf[s + 1], s1, 0, 0, 0);
  }
  return s0 + s1;
}
DI s16x4 soft_part(SW& c, const f32x4 S) {
  float mx = fmaxf(fmaxf(S[0], S[1]), fmaxf(S[2], S[3]));
  mx = xrow16_max(mx);
  if (__any(mx > c.m)) {
    const float mn = fmaxf(c.m, mx);
    const float alpha = __builtin_amdgcn_exp2f((c.m - mn) * C2);
    c.m = mn; c.l *= alpha;
#pragma unroll
    for (int cb = 0; cb < 16; ++cb) c.acc[cb] *= alpha;
  }
  const float mc = c.m * C2;
  const float p0 = __builtin_amdgcn_exp2f(fmaf(S[0], C2, -mc)), p1 = __builtin_amdgcn_exp2f(fmaf(S[1], C2, -mc));
  const float p2 = __builtin_amdgcn_exp2f(fmaf(S[2], C2, -mc)), p3 = __builtin_amdgcn_exp2f(fmaf(S[3], C2, -mc));
  c.l += (p0 + p1) + (p2 + p3);
  u32x2 pk = {cvtpk(p0, p1), cvtpk(p2, p3)};
  return __builtin_bit_cast(s16x4, pk);
}
DI void pv_part(SW& c, const s16x4 P, unsigned voff) {
  s16x4 vf[16];
#pragma unroll
  for (int cb = 0; cb < 16; ++cb) vf[cb] = __builtin_amdgcn_ds_read_tr16_b64_v4i16((LAS s16x4*)(size_t)(c.va[cb & 7] + voff + (cb >> 3) * 256u));
  __builtin_amdgcn_sched_barrier(0);
#pragma unroll
  for (int cb = 0; cb < 16; ++cb) c.acc[cb] = __builtin_amdgcn_mfma_f32_16x16x16bf16_1k(vf[cb], P, c.acc[cb], 0, 0, 0);
  __builtin_amdgcn_sched_barrier(0);
}

DI void wait_flag(volatile LAS unsigned* f, unsigned need) {
  unsigned spins = 0;
  while (*f < need) { __builtin_amdgcn_s_sleep(1); if (++spins > SPIN_L) break; }
  asm volatile("" ::: "memory");
}
template <int SLOT> DI void tile(SW& c, unsigned t, volatile LAS unsigned* flag, int wave) {
  wait_flag(flag + 14, 16u * t + 2u * wave + 2u);
  const f32x4 S = qk_part(c, SLOT * 8192u);
  const s16x4 P = soft_part(c, S);
  WAITL0();
  if ((threadIdx.x & 63) == 0) flag[4 + wave] = t + 1u;
  wait_flag(flag + 14, 16u * t + 2u * wave + 10u);
  pv_part(c, P, 16384u + SLOT * 8192u);
  WAITL0();
  if ((threadIdx.x & 63) == 0) flag[8 + wave] = t + 1u;
}

struct Args {
  const u16* kc; const u16* vc; const u16* x; const u16* wq; const u16* gw; unsigned char* ws; const u16* wo; u16* out;
  const u16* wg; const u16* wu; const u16* wd;
};

DI const CAS Args* kargs() {
  const CAS Args* p = (const CAS Args*)__builtin_amdgcn_kernarg_segment_ptr();
  asm volatile("" : "+s"(p));
  return p;
}

template <int NR> DI void colacc(const u32x4 (&w)[NR], const float (&xv)[NR], float* red, int lane) {
  float acc[8];
#pragma unroll
  for (int j = 0; j < 8; ++j) acc[j] = 0.f;
#pragma unroll
  for (int i = 0; i < NR; ++i)
#pragma unroll
    for (int j = 0; j < 4; ++j) { acc[2 * j] = fmaf(xv[i], lo16(w[i][j]), acc[2 * j]); acc[2 * j + 1] = fmaf(xv[i], hi16(w[i][j]), acc[2 * j + 1]); }
  *reinterpret_cast<f32x4*>(red + lane * 8) = f32x4{acc[0], acc[1], acc[2], acc[3]};
  *reinterpret_cast<f32x4*>(red + lane * 8 + 4) = f32x4{acc[4], acc[5], acc[6], acc[7]};
}
DI float red8(const float* red, int base, int stride) {
  float t = 0.f;
#pragma unroll
  for (int i = 0; i < 8; ++i) t += red[base + i * stride];
  return t;
}

#define NTHR 384
__global__ void __launch_bounds__(NTHR, 1) fused_kernel(Args a) {
  extern __shared__ __attribute__((aligned(16))) unsigned char lds[];
  {
    const GAS u16* p0 = (const GAS u16*)a.kc; const GAS u16* p1 = (const GAS u16*)a.vc; const GAS u16* p2 = (const GAS u16*)a.x;
    const GAS u16* p3 = (const GAS u16*)a.wq; const GAS u16* p4 = (const GAS u16*)a.gw; GAS unsigned char* p5 = (GAS unsigned char*)a.ws;
    asm volatile("" : "+s"(p0), "+s"(p1), "+s"(p2), "+s"(p3), "+s"(p4), "+s"(p5));
    a.kc = (const u16*)p0; a.vc = (const u16*)p1; a.x = (const u16*)p2; a.wq = (const u16*)p3; a.gw = (const u16*)p4; a.ws = (unsigned char*)p5;
  }
  const int b = blockIdx.x;
  const int h = (b >> 3) & 7, s = (b & 7) + 8 * (b >> 6);
  const int xh = b & 7, xs = b >> 3;
  const int me = h * 32 + s;
  const int tid = threadIdx.x, lane = tid & 63, wave = __builtin_amdgcn_readfirstlane(tid >> 6);
  volatile LAS unsigned* flag = (volatile LAS unsigned*)(size_t)((unsigned)(size_t)lds + L_FLAG);
  unsigned char* in2 = a.ws + WS_IN2;
  unsigned char* yb_ = a.ws + WS_Y;

  const unsigned ldsb = (unsigned)(size_t)lds;

  if (wave == NW) {
    __builtin_amdgcn_s_setprio(3);
    const unsigned tstart = now32();
    const int lc = lane < 56 ? lane : 55;
    const unsigned Ls = (3u - (unsigned)((size_t)a.wq >> 7)) & 7u;
    const bool wide = (unsigned)(xs >> 2) == Ls;
    const int r0 = wide ? 112 * (xs & 3) + 14 * (lane >> 3) : 14 * (lane >> 1);
    u32x4 wqr[14], xx, gg, xa, xb;
    {
      const int col = wide ? (xs >> 2) * 64 + (lane & 7) * 8 : xs * 16 + (lane & 1) * 8;
      const u16* wp = a.wq + (size_t)r0 * 4096 + (xh * 512 + col);
#pragma unroll
      for (int i = 0; i < 14; ++i) wqr[i] = LDW(wp + (size_t)i * 4096);
      xa = *reinterpret_cast<const u32x4_a4*>(a.x + r0);
      xb = *reinterpret_cast<const u32x4_a4*>(a.x + r0 + 6);
      xx = *reinterpret_cast<const u32x4*>(a.x + lc * 8); gg = *reinterpret_cast<const u32x4*>(a.gw + lc * 8);
    }
    if (lane < 32) flag[lane] = lane == 15 ? 0x7fffffffu : 0u;
    BAR();
    u64 E = *(const CAS u64*)(a.ws + WS_E);
    float xv[14];
#pragma unroll
    for (int i = 0; i < 14; ++i) asm volatile("" : "+v"(wqr[i]));
    asm volatile("" : "+v"(xa), "+v"(xb));
#pragma unroll
    for (int i = 0; i < 4; ++i) { xv[2 * i] = lo16(xa[i]); xv[2 * i + 1] = hi16(xa[i]); }
#pragma unroll
    for (int i = 0; i < 3; ++i) { xv[8 + 2 * i] = lo16(xb[1 + i]); xv[9 + 2 * i] = hi16(xb[1 + i]); }
    float* red = reinterpret_cast<float*>(lds + L_RED);
    asm volatile("" : "+s"(E));
    const u64 T = next_tag(E);
    const u32x2 Tw = {(unsigned)T, (unsigned)(T >> 32)};
    if (lane == 0) { flag[16] = Tw[0]; flag[17] = Tw[1]; }
    {
      colacc<14>(wqr, xv, red, lane);
      WAITL0();
      float qv = red8(red, wide ? lane : (lane >> 4) * 128 + (lane & 15), wide ? 64 : 16);
      if (wide) {
        st_sc1_x2(a.ws + WS_QP + (size_t)((xh * 4 + (xs & 3)) * 64 + lane) * 8, u32x2{__float_as_uint(qv), Tw[0]});
      } else {
        qv = xrow16_sum(qv);
        const u64 gr = (T << 16) | (u64)(cvtpk(qv, 0.f) & 0xffffu);
        if (lane < 16) st_sc1_x2(a.ws + WS_QG + (size_t)(xh * 512 + xs * 16 + lane) * 8, u32x2{(unsigned)gr, (unsigned)(gr >> 32)});
      }
    }
    if (lane < 56) *reinterpret_cast<u32x4*>(lds + L_X + lane * 16) = xx;
    bool dead = false;
    {
      const unsigned char* qg = a.ws + WS_QG + (size_t)h * 4096 + lane * 16;
      const unsigned char* qp = a.ws + WS_QP + (size_t)h * 2048 + lane * 16;
      const unsigned tlo = (unsigned)(T << 16), thi = (unsigned)(T >> 16);
      const unsigned mine = (unsigned)(lane >> 5);
      auto okg = [&](const u32x4& g, unsigned j) { return (((g[0] & 0xffff0000u) == tlo) & (g[1] == thi) & ((g[2] & 0xffff0000u) == tlo) & (g[3] == thi)) | (2 * j + mine == Ls); };
      auto okp = [&](const u32x4& g) { return ((g[1] == Tw[0]) & (g[3] == Tw[0])) | (Ls > 7u); };
      u32x4 g0, g1, g2, g3, p0, p1, e0, e1, e2, e3, f0, f1;
      while (now32() - tstart < (unsigned)QW0) __builtin_amdgcn_s_sleep(2);
#define QLOADS(a0, a1, a2, a3, b0, b1, tail)                                  \
      asm volatile("global_load_dwordx4 %0, %6, off sc1\n\t"                  \
                   "global_load_dwordx4 %1, %6, off offset:1024 sc1\n\t"      \
                   "global_load_dwordx4 %2, %6, off offset:2048 sc1\n\t"      \
                   "global_load_dwordx4 %3, %6, off offset:3072 sc1\n\t"      \
                   "global_load_dwordx4 %4, %7, off sc1\n\t"                  \
                   "global_load_dwordx4 %5, %7, off offset:1024 sc1" tail     \
                   : "=&v"(a0), "=&v"(a1), "=&v"(a2), "=&v"(a3), "=&v"(b0), "=&v"(b1) : "v"(qg), "v"(qp) : "memory")
      QLOADS(g0, g1, g2, g3, p0, p1, "");
      while (now32() - tstart < (unsigned)(QW0 + QD)) __builtin_amdgcn_s_sleep(1);
      QLOADS(e0, e1, e2, e3, f0, f1, "\n\ts_waitcnt vmcnt(6)");
      asm volatile("" : "+v"(g0), "+v"(g1), "+v"(g2), "+v"(g3), "+v"(p0), "+v"(p1));
      if (__any(!(okg(g0, 0) & okg(g1, 1) & okg(g2, 2) & okg(g3, 3) & okp(p0) & okp(p1)))) {
        WAITV(0);
        asm volatile("" : "+v"(e0), "+v"(e1), "+v"(e2), "+v"(e3), "+v"(f0), "+v"(f1));
        if (!okg(g0, 0)) g0 = e0;
        if (!okg(g1, 1)) g1 = e1;
        if (!okg(g2, 2)) g2 = e2;
        if (!okg(g3, 3)) g3 = e3;
        if (!okp(p0)) p0 = f0;
        if (!okp(p1)) p1 = f1;
        unsigned spins = 0;
        for (;;) {
          const bool need = !(okg(g0, 0) & okg(g1, 1) & okg(g2, 2) & okg(g3, 3) & okp(p0) & okp(p1));
          if (!__any(need)) break;
          if (++spins > SPIN_G) { dead = true; break; }
          if (spins > 1) { const unsigned t1 = now32(); while (now32() - t1 < (unsigned)QW1) __builtin_amdgcn_s_sleep(1); }
          if (need) {
            asm volatile("global_load_dwordx4 %0, %6, off sc1\n\t"
                         "global_load_dwordx4 %1, %6, off offset:1024 sc1\n\t"
                         "global_load_dwordx4 %2, %6, off offset:2048 sc1\n\t"
                         "global_load_dwordx4 %3, %6, off offset:3072 sc1\n\t"
                         "global_load_dwordx4 %4, %7, off sc1\n\t"
                         "global_load_dwordx4 %5, %7, off offset:1024 sc1\n\t"
                         "s_waitcnt vmcnt(0)"
                         : "+&v"(g0), "+&v"(g1), "+&v"(g2), "+&v"(g3), "+&v"(p0), "+&v"(p1) : "v"(qg), "v"(qp) : "memory");
          }
        }
      }
      unsigned* ql = reinterpret_cast<unsigned*>(lds + L_Q);
      ql[lane] = (g0[0] & 0xffffu) | (g0[2] << 16);
      ql[64 + lane] = (g1[0] & 0xffffu) | (g1[2] << 16);
      ql[128 + lane] = (g2[0] & 0xffffu) | (g2[2] << 16);
      ql[192 + lane] = (g3[0] & 0xffffu) | (g3[2] << 16);
      {
        const float qa = xhalf_sum(__uint_as_float(p0[0]) + __uint_as_float(p1[0]));
        const float qb = xhalf_sum(__uint_as_float(p0[2]) + __uint_as_float(p1[2]));
        if (lane < 32) ql[Ls * 32 + lane] = cvtpk(qa, qb);
      }
      WAITL0();
      if (lane == 0) flag[0] = 1u;
      WAITV(0);
      asm volatile("" :: "v"(e0), "v"(e1), "v"(e2), "v"(e3), "v"(f0), "v"(f1));
    }
    const CAS Args* ka = kargs();
    u32x4 wgr[7], wur[7], wdr[8];
    const int lg = b >> 3, ks = b & 7;
    if (b < NFF) {
      const size_t o = (size_t)(56 * ks + (lane >> 3)) * F + lg * 64 + (lane & 7) * 8;
#pragma unroll
      for (int i = 0; i < 7; ++i) { wgr[i] = LDW(ka->wg + o + (size_t)i * 8 * F); wur[i] = LDW(ka->wu + o + (size_t)i * 8 * F); }
#pragma unroll
      for (int u = 0; u < 8; ++u) wdr[u] = LDW(ka->wd + (size_t)(b * 8 + u) * D + lc * 8);
    }
    {
      const u32x4 e4 = {EMPTY, EMPTY, EMPTY, EMPTY};
      unsigned char* p = in2 + (size_t)me * IN2_STRIDE + lane * 16;
      st_sc1_x4(p, e4); st_sc1_x4(p + 1024, e4);
      st_sc1_x4(p + 2048, e4); st_sc1_x4(p + 3072, e4);
      unsigned char* q = yb_ + (size_t)b * 2048 + lane * 16;
      st_sc1_x4(q, e4);
      if (lane < 48) st_sc1_x4(q + 1024, e4);
    }
    {
      const int o0 = (2 * h + (s >> 4)) * 256 + (s & 15) * 16;
      const __amdgpu_buffer_rsrc_t rwo = __builtin_amdgcn_make_buffer_rsrc((void*)(ka->wo + (size_t)o0 * D), 0, 16 * D * 2, 0x00020000);
      const unsigned vl16 = lane * 16u;
#pragma unroll
      for (int k = 0; k < 14; ++k) DMA1(ldsb + L_W + k * 1024u, rwo, k * 1024u, vl16);
    }
    WAITV(0);
    if (lane == 0) st_sc1_x2(a.ws + WS_RDY + (size_t)b * 8, Tw);
    __builtin_amdgcn_s_setprio(0);
    {
      float t = 0.f;
#pragma unroll
      for (int j = 0; j < 4; ++j) { t += rbf(lo16(xx[j]) * lo16(gg[j])); t += rbf(hi16(xx[j]) * hi16(gg[j])); }
      if (lane >= 56) t = 0.f;
      t = rbf(wave_sum(t));
      const float g = rbf(1.f / (1.f + __expf(-t)));
      float yb[8];
#pragma unroll
      for (int j = 0; j < 8; ++j) yb[j] = 0.f;
      if (b < NFF) {
        float xr[7];
#pragma unroll
        for (int i = 0; i < 7; ++i) xr[i] = lo16(reinterpret_cast<const u16*>(lds + L_X)[56 * ks + 8 * i + (lane >> 3)]);
        colacc<7>(wgr, xr, red, lane);
        colacc<7>(wur, xr, red + 512, lane);
        WAITL0();
        const float pg = red8(red, lane, 64), pu = red8(red + 512, lane, 64);
        st_sc1_x4(a.ws + WS_FP + (size_t)(b * 64 + lane) * 16, u32x4{__float_as_uint(pg), Tw[0], __float_as_uint(pu), Tw[0]});
        const unsigned char* fp = a.ws + WS_FP + (size_t)((lg * 8 + (lane >> 3)) * 64 + ks * 8 + (lane & 7)) * 16;
        u32x4 f = {0u, ~Tw[0], 0u, ~Tw[0]};
        unsigned spins = 0;
        for (;;) {
          const bool need = (f[1] != Tw[0]) | (f[3] != Tw[0]);
          if (!__any(need)) break;
          if (dead || ++spins > SPIN_G) { dead = true; break; }
          { const unsigned t1 = now32(); while (now32() - t1 < 60u) __builtin_amdgcn_s_sleep(2); }
          if (need) asm volatile("global_load_dwordx4 %0, %1, off sc1\n\ts_waitcnt vmcnt(0)" : "+&v"(f) : "v"(fp) : "memory");
        }
        float gs = __uint_as_float(f[0]), us = __uint_as_float(f[2]);
        gs += dpp<ROR8>(gs); us += dpp<ROR8>(us);
        const float r = rbf(xrow16_sum(gs)), up = rbf(xrow16_sum(us));
        const float tt = tanhf(0.7978845608028654f * (r + 0.044715f * r * r * r));
        const float hv = rbf(rbf(0.5f * r * (1.f + tt)) * up);
#pragma unroll
        for (int u = 0; u < 8; ++u) {
          const float hu = __builtin_bit_cast(float, __builtin_amdgcn_readlane(__builtin_bit_cast(int, hv), u));
#pragma unroll
          for (int j = 0; j < 4; ++j) { yb[2 * j] = fmaf(hu, lo16(wdr[u][j]), yb[2 * j]); yb[2 * j + 1] = fmaf(hu, hi16(wdr[u][j]), yb[2 * j + 1]); }
        }
      }
      const float omg = rbf(1.f - g);
      float* yl = reinterpret_cast<float*>(lds + L_YB);
      if (lane < 56) {
        *reinterpret_cast<f32x4*>(yl + lane * 8) = f32x4{yb[0] * omg, yb[1] * omg, yb[2] * omg, yb[3] * omg};
        *reinterpret_cast<f32x4*>(yl + lane * 8 + 4) = f32x4{yb[4] * omg, yb[5] * omg, yb[6] * omg, yb[7] * omg};
      }
      if (lane == 0) yl[448] = g;
      WAITL0();
      if (lane == 0) flag[12] = 1u;
    }
    if (b == MASTER) {
      const unsigned char* rp = a.ws + WS_RDY + lane * 16;
      unsigned spins = 0;
      for (;;) {
        u32x4 q0, q1;
        asm volatile("global_load_dwordx4 %0, %2, off sc1\n\tglobal_load_dwordx4 %1, %2, off offset:1024 sc1\n\ts_waitcnt vmcnt(0)"
                     : "=&v"(q0), "=&v"(q1) : "v"(rp) : "memory");
        const unsigned tl = Tw[0], th = Tw[1];
        const bool ok = (q0[0] == tl) & (q0[1] == th) & (q0[2] == tl) & (q0[3] == th) & (q1[0] == tl) & (q1[1] == th) & (q1[2] == tl) & (q1[3] == th);
        if (__all(ok)) break;
        if (dead || ++spins > SPIN_G) { dead = true; break; }
        __builtin_amdgcn_s_sleep(8);
      }
      if (lane < 32) st_sc1_x2(a.ws + WS_BC + lane * 512, Tw);
    }
    {
      const unsigned char* pp = a.ws + WS_BC + (b & 31) * 512;
      unsigned spins = 0;
      for (;;) {
        u32x2 ent;
        asm volatile("global_load_dwordx2 %0, %1, off sc1\n\ts_waitcnt vmcnt(0)" : "=&v"(ent) : "v"(pp) : "memory");
        if (rfl(ent[0]) == Tw[0] && rfl(ent[1]) == Tw[1]) break;
        if (dead || ++spins > SPIN_G) break;
        { const unsigned t1 = now32(); while (now32() - t1 < 200u) __builtin_amdgcn_s_sleep(8); }
      }
      if (lane == 0) flag[13] = 1u;
    }
    {
      const bool rep = NREP == 32 ? ((b >> 3) & 7) == 0 : NREP == 16 ? ((b >> 3) & 15) == 5 : (b >> 3) == 13;
      const int ridx = NREP == 32 ? (b & 7) + 8 * (b >> 6) : NREP == 16 ? (b & 7) + 8 * (b >> 7) : (b & 7);
      constexpr int PC = 16 * CKR;
      int c = CK0, rd = -1;
      unsigned tc = 0, T0 = 0, Tf = 0;
      int cf = 0;
      bool have = false;
      const float phi = 2.f * (float)((b * 157) & 255) * (1.f / 256.f) - 1.f;
      float A0 = 0.f, rho = RHO0;
      for (unsigned spins = 0; spins < 4000u; ++spins) {
        const unsigned L = rfl(flag[14]);
        const unsigned t = now32();
        if (L >= (unsigned)(16 * NT - 12)) break;
        if (c < NT / CKR && L >= (unsigned)(PC * c)) {
          tc = t; rd = c; ++c;
          if (rep && lane < RSTR / 16) st_sc1_x4(a.ws + WS_PR + (size_t)rd * (32 * RSTR) + ridx * RSTR + lane * 16, u32x4{Tw[0], tc, Tw[0], tc});
        }
        if (rd >= 0 && (int)(t - tc) >= DLY) {
          if (have && lane == 0) flag[15] = (unsigned)(A0 + (float)(int)(t + 150u - T0) * rho);
          u32x2 ent;
          asm volatile("global_load_dwordx2 %0, %1, off sc1\n\ts_waitcnt vmcnt(0)" : "=&v"(ent)
                       : "v"(a.ws + WS_PR + (size_t)rd * (32 * RSTR) + (lane & (NREP - 1)) * RSTR) : "memory");
          const int dn = min((int)(now32() - tc), 2 * DLY);
          int d = (int)(ent[1] - tc);
          d = ent[0] == Tw[0] ? max(min(d, 1000), -1000) : dn;
          const int lead = (int)rfl((unsigned)(int)(wave_sum((float)d) * (1.f / 64.f)));
          const unsigned Tn = tc + (unsigned)lead;
          if (!have) { Tf = Tn; cf = rd; }
          else if (!FIXRHO) {
            const float per = fminf(fmaxf((float)(int)(Tn - Tf) / (float)(rd - cf), (float)PC * 13.f), (float)PC * 17.f);
            rho = (float)PC * ACC / per;
          }
          T0 = Tn; A0 = (float)(PC * rd + OFFP) + phi; have = true; rd = -1;
        } else {
          if (have && lane == 0) flag[15] = (unsigned)(A0 + (float)(int)(t - T0) * rho);
        }
        __builtin_amdgcn_s_sleep(1);
      }
      if (lane == 0) flag[15] = 0x7fffffffu;
    }
    return;
  }

  const unsigned hi = lane >> 5, pc = lane & 31;
  if (wave == NW + 1) {
    __builtin_amdgcn_s_setprio(2);
    const int row0 = ((s >> 3) & 1) | (((s & 7) | ((s >> 4) << 3)) << 6);
    const size_t base = ((size_t)h * SK + (size_t)row0 * (16 * NW)) * DH * 2;
    const int nrec = (SK - row0 * (16 * NW)) * DH * 2;
    const __amdgpu_buffer_rsrc_t rk = __builtin_amdgcn_make_buffer_rsrc((void*)((const char*)a.kc + base), 0, nrec, 0x00020000);
    const __amdgpu_buffer_rsrc_t rv = __builtin_amdgcn_make_buffer_rsrc((void*)((const char*)a.vc + base), 0, nrec, 0x00020000);
    unsigned vk[8], vv[8];
#pragma unroll
    for (int j = 0; j < 8; ++j) vk[j] = (hi * 512u + ((pc ^ hi) << 4)) ^ (unsigned)(j << 5);
#pragma unroll
    for (int j = 0; j < 8; ++j) vv[j] = (hi * 512u + ((pc ^ (hi << 1)) << 4)) ^ (unsigned)((j & 3) << 6);
    unsigned vt0;
    asm volatile("buffer_load_dword %0, %1, %2, 0 offen" : "=&v"(vt0) : "v"(0u), "s"(rv) : "memory");
    const unsigned fa = ldsb + L_FLAG + 14 * 4;
#define ROWI(t) (2u * (unsigned)(t))
    int L = 0;
#pragma unroll 1
    for (int t = 0; t < 2; ++t) {
      const unsigned lo0 = ldsb + L_RING + t * 8192u;
#pragma unroll 1
      for (int w = 0; w < NW; ++w) {
        if (t == 0 && w == EBW) BAR();
        const int g = 16 * t + 2 * w;
        const int l0 = max(L, g - (W0 - 1) / 4), l1 = max(L, g + 1 - (W0 - 1) / 4);
        dma_tile<W0>(lo0 + w * 32768u, rk, (ROWI(t) * NW + w) * 8192u, vk, fa, l0, l1);
        L = l1;
      }
#pragma unroll 1
      for (int w = 0; w < NW; ++w) {
        const int g = 16 * t + 8 + 2 * w;
        const int l0 = max(L, g - (W0 - 1) / 4), l1 = max(L, g + 1 - (W0 - 1) / 4);
        dma_tile<W0>(lo0 + w * 32768u + 16384u, rv, (ROWI(t) * NW + w) * 8192u, vv, fa, l0, l1);
        L = l1;
      }
    }
    unsigned fv = flag[4];
    unsigned av = flag[15];
    auto pace = [&](int g) -> bool {
      const unsigned a0 = rfl(av);
      if ((unsigned)g > a0) {
        unsigned sp = 0;
        while ((unsigned)g > rfl(flag[15])) { __builtin_amdgcn_s_sleep(1); if (++sp > 2000u) break; }
      }
      av = flag[15];
      return (a0 > (unsigned)(g + BTH)) & (a0 != 0x7fffffffu);
    };
    auto slot = [&](int j, int t, int g) {
      if (rfl(fv) < (unsigned)(t - 1)) {
        if (rfl(flag[4 + j]) < (unsigned)(t - 1)) {
          WAITV(0);
          L = g;
          if (lane == 0) flag[14] = (unsigned)L;
          wait_flag(flag + 4 + j, (unsigned)(t - 1));
        }
      }
      fv = flag[4 + ((j + 1) & 7)];
    };
#pragma unroll 1
    for (int t = 2; t < NT; ++t) {
      const unsigned lo0 = ldsb + L_RING + (t & 1) * 8192u;
#pragma unroll 1
      for (int w = 0; w < NW; ++w) {
        const int g = 16 * t + 2 * w;
        slot(w, t, g);
        if (pace(g)) tile_w<NKB>(lo0 + w * 32768u, rk, (ROWI(t) * NW + w) * 8192u, vk, fa, g, L); else
        tile_w<NK>(lo0 + w * 32768u, rk, (ROWI(t) * NW + w) * 8192u, vk, fa, g, L);
      }
#pragma unroll 1
      for (int w = 0; w < NW; ++w) {
        const int g = 16 * t + 8 + 2 * w;
        slot(4 + w, t, g);
        if (pace(g)) tile_w<NKB>(lo0 + w * 32768u + 16384u, rv, (ROWI(t) * NW + w) * 8192u, vv, fa, g, L); else
        tile_w<NK>(lo0 + w * 32768u + 16384u, rv, (ROWI(t) * NW + w) * 8192u, vv, fa, g, L);
      }
    }
    asm volatile("" :: "v"(vt0));
#pragma unroll
    for (int k = ((NKB > NK ? NKB : NK) + 2) / 4; k >= 0; --k) {
      asm volatile("s_waitcnt vmcnt(%0)" :: "n"(4 * k) : "memory");
      if (16 * NT - k > L) { L = 16 * NT - k; if (lane == 0) flag[14] = (unsigned)L; }
    }
    return;
  }

  SW c;
  const unsigned m0base = ldsb + L_RING + wave * 32768u;
  const unsigned cc = lane & 15, g = lane >> 4;
#pragma unroll
  for (int q = 0; q < 4; ++q) c.ka[q] = m0base + cc * 512u + ((g ^ (cc & 3)) << 4) + ((q ^ (cc >> 2)) << 6);
  {
    const unsigned qq = cc >> 2, p = cc & 3, t = 4 * g + qq;
#pragma unroll
    for (int e = 0; e < 8; ++e) c.va[e] = m0base + t * 512u + ((e ^ (t & 7)) << 5) + p * 8u;
  }
  const int SB = 16 + 12 * wave;
  BAR();
  wait_flag(flag, 1u);
#pragma unroll
  for (int st = 0; st < 8; ++st) {
    const u32x4 qr = *reinterpret_cast<const u32x4*>(lds + L_Q + (cc & 1) * 512 + 64 * st + 16 * g);
    const u32x4 z = {0u, 0u, 0u, 0u};
    c.qf[st] = __builtin_bit_cast(bf16x8, cc < 2 ? qr : z);
  }
  c.m = -INFINITY; c.l = 0.f;
#pragma unroll
  for (int cb = 0; cb < 16; ++cb) c.acc[cb] = f32x4{0.f, 0.f, 0.f, 0.f};
  unsigned wp2[8][2];
  f32x2 ybv = {0.f, 0.f};
  float gate = 0.f;
  const int cp = min(wave * 64 + lane, 223);
  const int blk = (wave & 1) * 8 + (lane >> 3);
  unsigned char* p_send = in2 + (size_t)(h * 32 + (wave >> 1) * 16 + blk) * IN2_STRIDE + s * 128 + (lane & 7) * 16;
  const unsigned char* p_in = in2 + (size_t)me * IN2_STRIDE + wave * 1024 + lane * 16;
  unsigned char* p_y = yb_ + (size_t)b * 2048 + cp * 8;
#pragma unroll
  for (int p = 0; p < 8; ++p) { wp2[p][0] = 0u; wp2[p][1] = 0u; }
#pragma unroll 1
  for (unsigned t = 0; t < (unsigned)NT; t += 2) {
    if (t == (unsigned)NT - 2) {
      wait_flag(flag + 12, 1u);
      wait_flag(flag + 13, 1u);
      const unsigned* wl = reinterpret_cast<const unsigned*>(lds + L_W) + cp;
#pragma unroll
      for (int p = 0; p < 8; ++p) {
        const unsigned r0 = wl[(2 * p) * 224], r1 = wl[(2 * p + 1) * 224];
        wp2[p][0] = __builtin_amdgcn_perm(r1, r0, 0x05040100u);
        wp2[p][1] = __builtin_amdgcn_perm(r1, r0, 0x07060302u);
      }
      ybv = *reinterpret_cast<const f32x2*>(lds + L_YB + cp * 8);
      gate = *reinterpret_cast<const float*>(lds + L_YB + 448 * 4);
      asm volatile("" : "+v"(p_send), "+v"(p_in), "+v"(p_y));
    }
    tile<0>(c, t, flag, wave);
    tile<1>(c, t + 1, flag, wave);
  }
  __builtin_amdgcn_s_setprio(3);
  const float lsum = xrow16_sum(c.l);
  {
    float* mrg = reinterpret_cast<float*>(lds + L_RING + wave * 32768u);
    if (cc < 2) {
#pragma unroll
      for (int cb = 0; cb < 16; ++cb) *reinterpret_cast<f32x4*>(mrg + cc * 260 + 4 + 16 * cb + 4 * g) = c.acc[cb];
      if (g == 0) *reinterpret_cast<f32x2*>(mrg + cc * 260) = f32x2{c.m, lsum};
    }
  }
  BAR();
  {
    const float* mp = reinterpret_cast<const float*>(lds + L_RING) + (wave >> 1) * 260;
    f32x2 mw[NW];
    f32x4 ow[NW];
#pragma unroll
    for (int w = 0; w < NW; ++w) {
      mw[w] = *reinterpret_cast<const f32x2*>(mp + w * 8192);
      ow[w] = *reinterpret_cast<const f32x4*>(mp + w * 8192 + 4 + blk * 16 + (lane & 3) * 4);
    }
    __builtin_amdgcn_sched_barrier(0);
    const float M = fmaxf(fmaxf(mw[0][0], mw[1][0]), fmaxf(mw[2][0], mw[3][0]));
    f32x4 o = {0.f, 0.f, 0.f, 0.f};
    float L = 0.f;
#pragma unroll
    for (int w = 0; w < NW; ++w) {
      const float wt = __builtin_amdgcn_exp2f((mw[w][0] - M) * C2);
      o += wt * ow[w];
      L = fmaf(wt, mw[w][1], L);
    }
    const bool sums = (lane & 4) == 0;
    st_sc1_x4(p_send, u32x4{guard(sums ? o[0] : M), guard(sums ? o[1] : L), guard(sums ? o[2] : M), guard(sums ? o[3] : L)});
  }
  {
    u32x4 x;
    for (unsigned spins = 0; spins < SPIN_G; ++spins) {
      asm volatile("global_load_dwordx4 %0, %1, off sc1\n\ts_waitcnt vmcnt(0)" : "=&v"(x) : "v"(p_in) : "memory");
      if (__builtin_amdgcn_ballot_w64(max(max(x[0], x[1]), max(x[2], x[3])) == EMPTY) == 0ull) break;
    }
    unsigned* st = reinterpret_cast<unsigned*>(lds + L_RING + 16384u);
    const int rec = wave * 8 + (lane >> 3);
    if ((lane & 4) == 0) {
#pragma unroll
      for (int j = 0; j < 4; ++j) st[((lane & 3) * 4 + j) * 36 + rec] = x[j];
    } else if ((lane & 7) == 4) {
      *reinterpret_cast<u32x2*>(st + 1024 + rec * 2) = u32x2{x[0], x[1]};
    }
    BAR();
    const int d = lane & 15, qq = lane >> 4;
    const f32x4* sf = reinterpret_cast<const f32x4*>(st);
    const f32x4 o0 = sf[d * 9 + qq * 2], o1 = sf[d * 9 + qq * 2 + 1];
    f32x4 ml[4];
#pragma unroll
    for (int i = 0; i < 4; ++i) ml[i] = sf[256 + qq * 4 + i];
    __builtin_amdgcn_sched_barrier(0);
    const float M = xrow16_max(fmaxf(fmaxf(fmaxf(ml[0][0], ml[0][2]), fmaxf(ml[1][0], ml[1][2])), fmaxf(fmaxf(ml[2][0], ml[2][2]), fmaxf(ml[3][0], ml[3][2]))));
    const float mc = -M * C2;
    float w8[8];
#pragma unroll
    for (int i = 0; i < 4; ++i) { w8[2 * i] = __builtin_amdgcn_exp2f(fmaf(ml[i][0], C2, mc)); w8[2 * i + 1] = __builtin_amdgcn_exp2f(fmaf(ml[i][2], C2, mc)); }
    const float L = xrow16_sum(((w8[0] * ml[0][1] + w8[1] * ml[0][3]) + (w8[2] * ml[1][1] + w8[3] * ml[1][3])) +
                               ((w8[4] * ml[2][1] + w8[5] * ml[2][3]) + (w8[6] * ml[3][1] + w8[7] * ml[3][3])));
    const float os = xrow16_sum(((w8[0] * o0[0] + w8[1] * o0[1]) + (w8[2] * o0[2] + w8[3] * o0[3])) +
                                ((w8[4] * o1[0] + w8[5] * o1[1]) + (w8[6] * o1[2] + w8[7] * o1[3])));
    const float on = os * __builtin_amdgcn_rcpf(L);
    const unsigned pk = cvtpk(on, dpp<0xF5>(on));
    float y0 = 0.f, y1 = 0.f;
#pragma unroll
    for (int p = 0; p < 8; ++p) {
      const unsigned op = (unsigned)__builtin_amdgcn_readlane((int)pk, 2 * p);
      y0 = dot2(wp2[p][0], op, y0); y1 = dot2(wp2[p][1], op, y1);
    }
    y0 = fmaf(gate, y0, ybv[0]); y1 = fmaf(gate, y1, ybv[1]);
    if (wave * 64 + lane < 224) st_sc1_x2(p_y, u32x2{guard(y0), guard(y1)});
  }
  if (b < NC3) {
    const unsigned char* p0 = yb_ + (size_t)(wave * 64 + (lane >> 2)) * 2048 + b * 64 + (lane & 3) * 16;
    const unsigned char* p1 = p0 + 32 * 2048;
    u32x4 y0, y1, y2, y3;
    const unsigned char* p2 = p0 + 16 * 2048;
    const unsigned char* p3 = p1 + 16 * 2048;
    auto allthere = [&]() {
      const unsigned mx = max(max(max(max(y0[0], y0[1]), max(y0[2], y0[3])), max(max(y1[0], y1[1]), max(y1[2], y1[3]))),
                              max(max(max(y2[0], y2[1]), max(y2[2], y2[3])), max(max(y3[0], y3[1]), max(y3[2], y3[3]))));
      return __builtin_amdgcn_ballot_w64(mx == EMPTY) == 0ull;
    };
    for (unsigned spins = 0; spins < SPIN_G; ++spins) {
      asm volatile("global_load_dwordx4 %0, %4, off sc1\n\t"
                   "global_load_dwordx4 %1, %5, off sc1\n\t"
                   "global_load_dwordx4 %2, %6, off sc1\n\t"
                   "global_load_dwordx4 %3, %7, off sc1\n\t"
                   "s_waitcnt vmcnt(0)"
                   : "=&v"(y0), "=&v"(y1), "=&v"(y2), "=&v"(y3) : "v"(p0), "v"(p1), "v"(p2), "v"(p3) : "memory");
      if (allthere()) break;
    }
    f32x4 v;
#pragma unroll
    for (int j = 0; j < 4; ++j) {
      float t = (__uint_as_float(y0[j]) + __uint_as_float(y1[j])) + (__uint_as_float(y2[j]) + __uint_as_float(y3[j]));
      t += dpp<ROR4>(t); t += dpp<ROR8>(t);
      v[j] = xrow16_sum(t);
    }
    f32x4* fin = reinterpret_cast<f32x4*>(lds + L_RED);
    if (lane < 4) fin[wave * 4 + lane] = v;
    BAR();
    if (wave == 0 && lane < 4) {
      const f32x4 r = (fin[lane] + fin[4 + lane]) + (fin[8 + lane] + fin[12 + lane]);
      *reinterpret_cast<u32x2*>(a.out + b * 16 + lane * 4) = u32x2{cvtpk(r[0], r[1]), cvtpk(r[2], r[3])};
      if (b == 0 && lane == 0) st_sc1_x2(a.ws + WS_E, u32x2{flag[16], flag[17]});
    }
  }
}

extern "C" void kernel_launch(void* const* d_in, const int* in_sizes, int n_in, void* d_out, int out_size, void* d_ws, size_t ws_size, hipStream_t stream) {
  Args a;
  a.x = (const u16*)d_in[0]; a.kc = (const u16*)d_in[1]; a.vc = (const u16*)d_in[2]; a.wq = (const u16*)d_in[3]; a.wo = (const u16*)d_in[4];
  a.wg = (const u16*)d_in[6]; a.wu = (const u16*)d_in[7]; a.wd = (const u16*)d_in[8]; a.gw = (const u16*)d_in[9];
  a.out = (u16*)d_out; a.ws = (unsigned char*)d_ws;
  (void)hipFuncSetAttribute((const void*)fused_kernel, hipFuncAttributeMaxDynamicSharedMemorySize, LDS_BYTES);
  fused_kernel<<<NWG, NTHR, LDS_BYTES, stream>>>(a);
}
```
